```python
import torch

def _softmax(x, axis=-1):
    return torch.softmax(x, dim=axis)
def _gelu(x):
    return torch.nn.functional.gelu(x, approximate="tanh")
def _silu(x):
    return torch.nn.functional.silu(x)
def _sigmoid(x):
    return torch.sigmoid(x)
def _layernorm(x, axis=-1, eps=1e-5):
    m = x.mean(dim=axis, keepdim=True)
    v = x.var(dim=axis, keepdim=True, unbiased=False)
    return (x - m) / torch.sqrt(v + eps)
def _scatter_add(tgt, idx, src, axis=0):
    return tgt.clone().index_add_(axis, idx, src)
def _segment_sum(src, seg, n):
    out = torch.zeros((n,) + src.shape[1:], dtype=src.dtype)
    return out.index_add_(0, seg, src)
def _pad(x, pw):
    flat = tuple(v for pair in reversed(pw) for v in pair)
    return torch.nn.functional.pad(x, flat)

kernel_name = 'sequential_preLN__compressed_kv_cla__dense_activation_gelu__decode'

def setup_inputs():

    _w = (lambda a: (lambda t: t.to(torch.bfloat16) if t.is_floating_point() else t)(torch.as_tensor(a)))
    def _A_setup():
        B, H, Sq, Sk, D, M = 1, 32, 1, 16384, 128, 512
        Dc = 4 * D
        return {
            "x": _w((lambda *s: torch.randn(*s, dtype=torch.bfloat16) * 0.05)(B, Sq, M)),
            "c_kv": _w((lambda *s: torch.randn(*s, dtype=torch.bfloat16) * 0.8)(B, Sk, Dc)),
            "w_q": _w((lambda *s: torch.randn(*s, dtype=torch.bfloat16) * 3.2)(M, H, D) * (M ** -0.5)),
            "w_uk": _w((lambda *s: torch.randn(*s, dtype=torch.bfloat16) * 3.2)(Dc, H, D) * (Dc ** -0.5)),
            "w_uv": _w((lambda *s: torch.randn(*s, dtype=torch.bfloat16) * 0.8)(Dc, H, D) * (Dc ** -0.5)),
            "w_o": _w((lambda *s: torch.randn(*s, dtype=torch.bfloat16) * 0.8)(H, D, M) * ((H * D) ** -0.5)),
        }
    _A_in = _A_setup()

    def _B_setup():
        B, S, D, F = 1, 1, 512, 1792
        return {
            "x": _w((lambda *s: torch.randn(*s, dtype=torch.bfloat16) * 0.05)(B, S, D)),
            "w1": _w((lambda *s: torch.randn(*s, dtype=torch.bfloat16) * 3.2)(D, F) * (D ** -0.5)),
            "b1": _w((lambda *s: torch.randn(*s, dtype=torch.bfloat16) * 3.2)(F) * 0.01),
            "w2": _w((lambda *s: torch.randn(*s, dtype=torch.bfloat16) * 0.8)(F, D) * (F ** -0.5)),
            "b2": _w((lambda *s: torch.randn(*s, dtype=torch.bfloat16) * 0.8)(D) * 0.01),
        }
    _B_in = _B_setup()

    return {
        "A_x": _A_in["x"],
        "A_c_kv": _A_in["c_kv"],
        "A_w_q": _A_in["w_q"],
        "A_w_uk": _A_in["w_uk"],
        "A_w_uv": _A_in["w_uv"],
        "A_w_o": _A_in["w_o"],
        "B_x": _B_in["x"],
        "B_w1": _B_in["w1"],
        "B_b1": _B_in["b1"],
        "B_w2": _B_in["w2"],
        "B_b2": _B_in["b2"],
    }

def reference(A_x, A_c_kv, A_w_q, A_w_uk, A_w_uv, A_w_o, B_x, B_w1, B_b1, B_w2, B_b2):
    def _A_ref(x, c_kv, w_q, w_uk, w_uv, w_o):
        B, Sq, M = x.shape[0], x.shape[1], x.shape[2]
        Sk = c_kv.shape[1]
        H, D = w_q.shape[1], w_q.shape[2]
        q = torch.einsum("bqm,mhd->bhqd", x, w_q)
        k = torch.einsum("bkc,chd->bhkd", c_kv, w_uk)
        v = torch.einsum("bkc,chd->bhkd", c_kv, w_uv)
        scores = torch.einsum("bhqd,bhkd->bhqk", q, k) * (D ** -0.5)
        off = Sk - Sq
        qi = torch.arange(Sq)[:, None]
        kj = torch.arange(Sk)[None, :]
        cmask = kj <= (qi + off)
        scores = torch.where(cmask[None, None], scores, -1e9)
        p = _softmax(scores, axis=-1)
        ctx = torch.einsum("bhqk,bhkd->bhqd", p, v)
        return torch.einsum("bhqd,hdm->bqm", ctx, w_o)

    def _B_ref(x, w1, b1, w2, b2):
        h = torch.einsum("bsd,df->bsf", x, w1) + b1
        h = _gelu(h)
        return torch.einsum("bsf,fd->bsd", h, w2) + b2

    x = A_x
    h = x + _A_ref(_layernorm(x, axis=-1), A_c_kv, A_w_q, A_w_uk, A_w_uv, A_w_o)
    h2 = h + _B_ref(_layernorm(h, axis=-1), B_w1, B_b1, B_w2, B_b2)
    return h2
```

```cpp
#include <hip/hip_runtime.h>
#include <hip/hip_bf16.h>
#include <stdint.h>

typedef unsigned short u16;
typedef unsigned v4u __attribute__((ext_vector_type(4)));

__device__ __forceinline__ float bf2f(u16 b) { return __uint_as_float(((unsigned)b) << 16); }
__device__ __forceinline__ float bflo(unsigned u) { return __uint_as_float(u << 16); }
__device__ __forceinline__ float bfhi(unsigned u) { return __uint_as_float(u & 0xffff0000u); }
__device__ __forceinline__ u16 f2bf(float f) { __hip_bfloat16 b = __float2bfloat16(f); return *reinterpret_cast<u16*>(&b); }

constexpr int H = 32, D = 128, M = 512, DC = 512, SK = 16384, F = 1792;

__device__ __forceinline__ float block_sum(float v, float* red, int nthreads) {
    for (int o = 32; o > 0; o >>= 1) v += __shfl_xor(v, o);
    int w = threadIdx.x >> 6;
    __syncthreads();
    if ((threadIdx.x & 63) == 0) red[w] = v;
    __syncthreads();
    float s = 0.f;
    for (int i = 0; i < nthreads / 64; ++i) s += red[i];
    return s;
}

__global__ void __launch_bounds__(256) k1(const u16* x, const u16* wq, const u16* wuk, float* qa) {
    __shared__ float xn[M];
    __shared__ float red[8];
    __shared__ float qp[16][D];
    __shared__ float q[D];
    int h = blockIdx.x >> 3, part = blockIdx.x & 7;
    int t = threadIdx.x;
    float x0 = bf2f(x[t]), x1 = bf2f(x[t + 256]);
    float mean = block_sum(x0 + x1, red, 256) / M;
    float d0 = x0 - mean, d1 = x1 - mean;
    float var = block_sum(d0 * d0 + d1 * d1, red, 256) / M;
    float rs = rsqrtf(var + 1e-5f);
    xn[t] = d0 * rs; xn[t + 256] = d1 * rs;
    __syncthreads();
    int r = t >> 4, i = t & 15;
    float acc[8] = {0};
    for (int n = 0; n < 32; ++n) {
        int m = r + 16 * n;
        v4u w = *(const v4u*)(wq + (size_t)m * H * D + h * D + 8 * i);
        float xv = xn[m];
        for (int j = 0; j < 4; ++j) { acc[2 * j] += xv * bflo(w[j]); acc[2 * j + 1] += xv * bfhi(w[j]); }
    }
    for (int j = 0; j < 8; ++j) qp[r][8 * i + j] = acc[j];
    __syncthreads();
    if (t < D) { float s = 0; for (int rr = 0; rr < 16; ++rr) s += qp[rr][t]; q[t] = s; }
    __syncthreads();
    for (int n = 0; n < 4; ++n) {
        int c = part * 64 + r + 16 * n;
        v4u w = *(const v4u*)(wuk + (size_t)c * H * D + h * D + 8 * i);
        float s = 0;
        for (int j = 0; j < 4; ++j) { s += q[8 * i + 2 * j] * bflo(w[j]) + q[8 * i + 2 * j + 1] * bfhi(w[j]); }
        for (int o = 8; o > 0; o >>= 1) s += __shfl_xor(s, o);
        if (i == 0) qa[h * DC + c] = s * 0.08838834764831845f;
    }
}

__global__ void __launch_bounds__(256) k2(const u16* ckv, const float* qa, float* po, float* pm, float* pl) {
    __shared__ u16 ct[64][DC + 8];
    __shared__ float s[H][64];
    __shared__ float qs[DC];
    int b = blockIdx.x, t = threadIdx.x;
    for (int n = 0; n < 16; ++n) {
        int idx = t + 256 * n; int row = idx >> 6, ch = idx & 63;
        *(v4u*)&ct[row][ch * 8] = *(const v4u*)(ckv + ((size_t)b * 64 + row) * DC + ch * 8);
    }
    __syncthreads();
    int key = t & 63, hg = t >> 6;
    for (int hh = 0; hh < 8; ++hh) {
        int h = hg * 8 + hh;
        float a = 0;
        for (int c = 0; c < DC; c += 8) {
            v4u w = *(const v4u*)&ct[key][c];
            const float* qq = qa + h * DC + c;
            for (int j = 0; j < 4; ++j) a += qq[2 * j] * bflo(w[j]) + qq[2 * j + 1] * bfhi(w[j]);
        }
        float m = a;
        for (int o = 32; o > 0; o >>= 1) m = fmaxf(m, __shfl_xor(m, o));
        float p = __expf(a - m);
        float l = p;
        for (int o = 32; o > 0; o >>= 1) l += __shfl_xor(l, o);
        s[h][key] = p;
        if (key == 0) { pm[b * H + h] = m; pl[b * H + h] = l; }
    }
    __syncthreads();
    for (int h = 0; h < H; ++h) {
        float a0 = 0, a1 = 0;
        for (int k = 0; k < 64; ++k) {
            unsigned w = *(const unsigned*)&ct[k][2 * t];
            float p = s[h][k];
            a0 += p * bflo(w); a1 += p * bfhi(w);
        }
        po[((size_t)b * H + h) * DC + 2 * t] = a0;
        po[((size_t)b * H + h) * DC + 2 * t + 1] = a1;
    }
}

__global__ void __launch_bounds__(512) k3(const float* po, const float* pm, const float* pl, const u16* wuv, const u16* wo, float* op) {
    __shared__ float wgt[256];
    __shared__ float cc[DC];
    __shared__ float red[8];
    __shared__ float cp[32][D];
    __shared__ float ctx[D];
    int h = blockIdx.x, t = threadIdx.x;
    float m = t < 256 ? pm[t * H + h] : -1e30f;
    float mm = m;
    for (int o = 32; o > 0; o >>= 1) mm = fmaxf(mm, __shfl_xor(mm, o));
    if ((t & 63) == 0) red[t >> 6] = mm;
    __syncthreads();
    float gm = red[0]; for (int i = 1; i < 8; ++i) gm = fmaxf(gm, red[i]);
    float e = t < 256 ? __expf(m - gm) : 0.f;
    float l = t < 256 ? e * pl[t * H + h] : 0.f;
    float L = block_sum(l, red, 512);
    if (t < 256) wgt[t] = e / L;
    __syncthreads();
    float a = 0;
    for (int b = 0; b < 256; ++b) a += wgt[b] * po[((size_t)b * H + h) * DC + t];
    cc[t] = a;
    __syncthreads();
    int r = t >> 4, i = t & 15;
    float acc[8] = {0};
    for (int n = 0; n < 16; ++n) {
        int c = r + 32 * n;
        v4u w = *(const v4u*)(wuv + (size_t)c * H * D + h * D + 8 * i);
        float xv = cc[c];
        for (int j = 0; j < 4; ++j) { acc[2 * j] += xv * bflo(w[j]); acc[2 * j + 1] += xv * bfhi(w[j]); }
    }
    for (int j = 0; j < 8; ++j) cp[r][8 * i + j] = acc[j];
    __syncthreads();
    if (t < D) { float s = 0; for (int rr = 0; rr < 32; ++rr) s += cp[rr][t]; ctx[t] = s; }
    __syncthreads();
    float o = 0;
    for (int d = 0; d < D; ++d) o += ctx[d] * bf2f(wo[((size_t)h * D + d) * M + t]);
    op[h * M + t] = o;
}

__global__ void __launch_bounds__(512) k4(const u16* x, const float* op, const u16* w1, const u16* b1, float* hbuf, float* fbuf) {
    __shared__ float hn[M];
    __shared__ float red[8];
    __shared__ float fp[64][64];
    int t = threadIdx.x;
    float hv = bf2f(x[t]);
    for (int h = 0; h < H; ++h) hv += op[h * M + t];
    if (blockIdx.x == 0) hbuf[t] = hv;
    float mean = block_sum(hv, red, 512) / M;
    float d0 = hv - mean;
    float var = block_sum(d0 * d0, red, 512) / M;
    hn[t] = d0 * rsqrtf(var + 1e-5f);
    __syncthreads();
    int r = t >> 3, i = t & 7;
    float acc[8] = {0};
    for (int n = 0; n < 8; ++n) {
        int m = r + 64 * n;
        v4u w = *(const v4u*)(w1 + (size_t)m * F + blockIdx.x * 64 + 8 * i);
        float xv = hn[m];
        for (int j = 0; j < 4; ++j) { acc[2 * j] += xv * bflo(w[j]); acc[2 * j + 1] += xv * bfhi(w[j]); }
    }
    for (int j = 0; j < 8; ++j) fp[r][8 * i + j] = acc[j];
    __syncthreads();
    if (t < 64) {
        float s = bf2f(b1[blockIdx.x * 64 + t]);
        for (int rr = 0; rr < 64; ++rr) s += fp[rr][t];
        float u = 0.7978845608028654f * (s + 0.044715f * s * s * s);
        fbuf[blockIdx.x * 64 + t] = 0.5f * s * (1.f + tanhf(u));
    }
}

__global__ void __launch_bounds__(512) k5(const float* hbuf, const float* fbuf, const u16* w2, const u16* b2, u16* out) {
    __shared__ float fp[64][64];
    int t = threadIdx.x;
    int r = t >> 3, i = t & 7;
    float acc[8] = {0};
    for (int n = 0; n < 28; ++n) {
        int f = r + 64 * n;
        v4u w = *(const v4u*)(w2 + (size_t)f * M + blockIdx.x * 64 + 8 * i);
        float xv = fbuf[f];
        for (int j = 0; j < 4; ++j) { acc[2 * j] += xv * bflo(w[j]); acc[2 * j + 1] += xv * bfhi(w[j]); }
    }
    for (int j = 0; j < 8; ++j) fp[r][8 * i + j] = acc[j];
    __syncthreads();
    if (t < 64) {
        int m = blockIdx.x * 64 + t;
        float s = bf2f(b2[m]) + hbuf[m];
        for (int rr = 0; rr < 64; ++rr) s += fp[rr][t];
        out[m] = f2bf(s);
    }
}

extern "C" void kernel_launch(void* const* d_in, const int* in_sizes, int n_in,
                              void* d_out, int out_size, void* d_ws, size_t ws_size,
                              hipStream_t stream) {
    const u16* x = (const u16*)d_in[0];
    const u16* ckv = (const u16*)d_in[1];
    const u16* wq = (const u16*)d_in[2];
    const u16* wuk = (const u16*)d_in[3];
    const u16* wuv = (const u16*)d_in[4];
    const u16* wo = (const u16*)d_in[5];
    const u16* w1 = (const u16*)d_in[7];
    const u16* b1 = (const u16*)d_in[8];
    const u16* w2 = (const u16*)d_in[9];
    const u16* b2 = (const u16*)d_in[10];
    float* ws = (float*)d_ws;
    float* qa = ws;
    float* pm = qa + H * DC;
    float* pl = pm + 256 * H;
    float* op = pl + 256 * H;
    float* hb = op + H * M;
    float* fb = hb + M;
    float* po = fb + 2048;
    k1<<<256, 256, 0, stream>>>(x, wq, wuk, qa);
    k2<<<256, 256, 0, stream>>>(ckv, qa, po, pm, pl);
    k3<<<32, 512, 0, stream>>>(po, pm, pl, wuv, wo, op);
    k4<<<28, 512, 0, stream>>>(x, op, w1, b1, hb, fb);
    k5<<<8, 512, 0, stream>>>(hb, fb, w2, b2, (u16*)d_out);
}
```

```cpp
#include <hip/hip_runtime.h>
#include <stdint.h>

typedef unsigned short u16;
typedef unsigned long long u64;
typedef unsigned v4u __attribute__((ext_vector_type(4)));
typedef unsigned v2u __attribute__((ext_vector_type(2)));
typedef float f32x4 __attribute__((ext_vector_type(4)));
typedef float f32x2 __attribute__((ext_vector_type(2)));
typedef __bf16 bf16x8 __attribute__((ext_vector_type(8)));
typedef __bf16 bf16x2 __attribute__((ext_vector_type(2)));
typedef short s16x4 __attribute__((ext_vector_type(4)));
#define GAS __attribute__((address_space(1)))
#define LAS __attribute__((address_space(3)))
#define DI __device__ __forceinline__

#define STAMPS 0
#define DLY_B 24
#define DLY_V 8
#define DLY_C 36
#define DLY_D 8

constexpr int NWG = 256, NT = 768;
constexpr int W_EP = 0, W_FL = 4096, W_QA = 65536, W_LS = W_QA + 65536, W_CX = W_LS + 32768, W_OP = W_CX + 131072,
              W_YP = W_OP + 16384, W_SMALL_END = W_LS + 128 * 3072, W_OB = 1 << 20, W_END = W_OB + (4 << 20), W_ST = 8 << 20;
static_assert(W_YP + 16 * 56 * 128 <= W_SMALL_END && W_SMALL_END <= W_OB, "ws map");
constexpr int RS = 1040, L_CT = 0, L_QA = 128 * RS, L_P = L_QA + 16 * RS, PS = 272, L_MX = L_P + 16 * PS, L_LS = L_MX + 512,
              L_SC = L_LS + 512, L_END = L_SC + 4096;
constexpr unsigned SENT = 0xFFFFFFFFu;
constexpr unsigned SPIN_CAP = 200000;

struct Params {
    const u16 *x, *ckv, *wq, *wuk, *wuv, *wo, *w1, *b1, *w2, *b2;
    u16* out;
    unsigned char* ws;
};

DI float bflo(unsigned u) { return __uint_as_float(u << 16); }
DI float bfhi(unsigned u) { return __uint_as_float(u & 0xffff0000u); }
DI unsigned pk(float a, float b) { f32x2 f = {a, b}; return __builtin_bit_cast(unsigned, __builtin_convertvector(f, bf16x2)); }
DI unsigned san(unsigned u) { return u < 0xFFFFFFFEu ? u : 0xFFFFFFFEu; }
DI f32x4 mfma(v4u a, v4u b, f32x4 c) { return __builtin_amdgcn_mfma_f32_16x16x32_bf16(__builtin_bit_cast(bf16x8, a), __builtin_bit_cast(bf16x8, b), c, 0, 0, 0); }
template <int CTRL> DI float dpp(float v) { return __int_as_float(__builtin_amdgcn_update_dpp(0, __float_as_int(v), CTRL, 0xf, 0xf, true)); }
template <int CTRL> DI unsigned dppu(unsigned v) { return (unsigned)__builtin_amdgcn_update_dpp(0, (int)v, CTRL, 0xf, 0xf, true); }
DI float x16_sum(float v) { auto r = __builtin_amdgcn_permlane16_swap(__float_as_uint(v), __float_as_uint(v), false, false); return __uint_as_float(r[0]) + __uint_as_float(r[1]); }
DI float x32_sum(float v) { auto r = __builtin_amdgcn_permlane32_swap(__float_as_uint(v), __float_as_uint(v), false, false); return __uint_as_float(r[0]) + __uint_as_float(r[1]); }
DI float x16_max(float v) { auto r = __builtin_amdgcn_permlane16_swap(__float_as_uint(v), __float_as_uint(v), false, false); return fmaxf(__uint_as_float(r[0]), __uint_as_float(r[1])); }
DI float x32_max(float v) { auto r = __builtin_amdgcn_permlane32_swap(__float_as_uint(v), __float_as_uint(v), false, false); return fmaxf(__uint_as_float(r[0]), __uint_as_float(r[1])); }
DI float wave_sum(float v) {
    v += dpp<0xB1>(v); v += dpp<0x4E>(v); v += dpp<0x141>(v); v += dpp<0x140>(v);
    return x32_sum(x16_sum(v));
}
DI float wave_max(float v) {
    v = fmaxf(v, dpp<0xB1>(v)); v = fmaxf(v, dpp<0x4E>(v)); v = fmaxf(v, dpp<0x141>(v)); v = fmaxf(v, dpp<0x140>(v));
    return x32_max(x16_max(v));
}
DI void red2_rows(float& a, float& b) {
    auto r = __builtin_amdgcn_permlane32_swap(__float_as_uint(a), __float_as_uint(b), false, false);
    float s = __uint_as_float(r[0]) + __uint_as_float(r[1]);
    s = x16_sum(s);
    auto q = __builtin_amdgcn_permlane32_swap(__float_as_uint(s), __float_as_uint(s), false, false);
    a = __uint_as_float(q[0]); b = __uint_as_float(q[1]);
}
DI float sum4(f32x4 v) { return (v[0] + v[1]) + (v[2] + v[3]); }
DI unsigned umax4(v4u v) { return max(max(v[0], v[1]), max(v[2], v[3])); }
#define BAR() do { asm volatile("s_waitcnt lgkmcnt(0)" ::: "memory"); __builtin_amdgcn_s_barrier(); asm volatile("" ::: "memory"); } while (0)
#define FENCE() asm volatile("" ::: "memory")
typedef __amdgpu_buffer_rsrc_t rsrc_t;
DI v4u ld128(rsrc_t r, int off) { return __builtin_amdgcn_raw_buffer_load_b128(r, off, 0, 16); }
DI v2u ld64(rsrc_t r, int off) { return __builtin_amdgcn_raw_buffer_load_b64(r, off, 0, 16); }
DI unsigned ld32(rsrc_t r, int off) { return __builtin_amdgcn_raw_buffer_load_b32(r, off, 0, 16); }
DI void st128(rsrc_t r, int off, v4u v) { __builtin_amdgcn_raw_buffer_store_b128(v, r, off, 0, 16); }
DI void st64(rsrc_t r, int off, v2u v) { __builtin_amdgcn_raw_buffer_store_b64(v, r, off, 0, 16); }
DI void st32(rsrc_t r, int off, unsigned v) { __builtin_amdgcn_raw_buffer_store_b32(v, r, off, 0, 16); }
DI unsigned perm_lo(unsigned a, unsigned b) { return __builtin_amdgcn_perm(b, a, 0x05040100u); }
DI unsigned perm_hi(unsigned a, unsigned b) { return __builtin_amdgcn_perm(b, a, 0x07060302u); }

#define STAMP(i)
#define STAMP_OUT()

__global__ void __launch_bounds__(NT, 3) fused_k(Params p) {
    extern __shared__ __attribute__((aligned(16))) unsigned char lds_[];
    LAS unsigned char* L = (LAS unsigned char*)lds_;
    const int b = blockIdx.x, tid = threadIdx.x, l = tid & 63, w = __builtin_amdgcn_readfirstlane(tid >> 6);
    const int g = l >> 4, i16 = l & 15;
    const int x = b & 7, i = b >> 3, isW = i & 1, pi = i >> 1, kc = x * 16 + pi, hh = isW;
    const rsrc_t rs = __builtin_amdgcn_make_buffer_rsrc(p.ws, 0, W_END, 0x00020000);
    STAMP(0);
    v4u selm;
#pragma unroll
    for (int t = 0; t < 4; ++t) selm[t] = (((l & 7) >> 1) == t) ? ((l & 1) ? 0xffff0000u : 0x0000ffffu) : 0u;

    if (w < 8) {
        if (isW) {
            const int hq = x * 4 + (pi >> 2), dh = (pi >> 1) & 1, ch = pi & 1;
            const int r8 = l >> 3, p8 = l & 7, wr = w ^ (4 * ch);
            const v4u xs = *(const GAS v4u*)(p.x + 8 * l);
            const v4u xr = *(const GAS v4u*)(p.x + 64 * wr + 8 * r8);
            v4u wq[8], uk[2][2];
            const GAS u16* wqp = (const GAS u16*)p.wq + (size_t)(64 * wr + 8 * r8) * 4096 + hq * 128 + dh * 64 + 8 * p8;
#pragma unroll
            for (int n = 0; n < 8; ++n) wq[n] = *(const GAS v4u*)(wqp + n * 4096);
            const GAS u16* ukp = (const GAS u16*)p.wuk + (size_t)(256 * ch + 32 * w + 2 * i16) * 4096 + hq * 128 + dh * 64 + 8 * g;
#pragma unroll
            for (int tt = 0; tt < 2; ++tt)
#pragma unroll
                for (int s = 0; s < 2; ++s) uk[tt][s] = *(const GAS v4u*)(ukp + tt * 4096 + 32 * s);
            __builtin_amdgcn_sched_barrier(0);
            float xv[8];
#pragma unroll
            for (int t = 0; t < 4; ++t) { xv[2 * t] = bflo(xs[t]); xv[2 * t + 1] = bfhi(xs[t]); }
            float sm = 0.f;
#pragma unroll
            for (int t = 0; t < 8; ++t) sm += xv[t];
            const float mean = wave_sum(sm) * (1.f / 512.f);
            float sq = 0.f;
#pragma unroll
            for (int t = 0; t < 8; ++t) { const float d = xv[t] - mean; sq += d * d; }
            const float rstd = __builtin_amdgcn_rsqf(wave_sum(sq) * (1.f / 512.f) + 1e-5f);
            f32x4 acc = {0.f, 0.f, 0.f, 0.f};
#pragma unroll
            for (int n = 0; n < 8; ++n) {
                const float xn = (((n & 1) ? bfhi(xr[n >> 1]) : bflo(xr[n >> 1])) - mean) * rstd;
                const unsigned dup = pk(xn, xn);
                const v4u a = {dup & selm[0], dup & selm[1], dup & selm[2], dup & selm[3]};
                acc = mfma(a, wq[n], acc);
            }
            if ((g >> 1) == (i16 >> 3)) {
                v2u o = {pk(acc[0], acc[1]), pk(acc[2], acc[3])};
                *(LAS v2u*)(L + L_SC + (2 * w + (g >> 1)) * 144 + (8 * (i16 & 7) + 4 * (g & 1)) * 2) = o;
            }
            STAMP(1);
            BAR();
            const v4u a0 = *(const LAS v4u*)(L + L_SC + i16 * 144 + 16 * g), a1 = *(const LAS v4u*)(L + L_SC + i16 * 144 + 64 + 16 * g);
            f32x4 d0 = {0.f, 0.f, 0.f, 0.f}, d1 = {0.f, 0.f, 0.f, 0.f};
            d0 = mfma(a0, uk[0][0], d0); d1 = mfma(a0, uk[1][0], d1);
            d0 = mfma(a1, uk[0][1], d0); d1 = mfma(a1, uk[1][1], d1);
            float r0 = sum4(d0), r1 = sum4(d1);
            red2_rows(r0, r1);
            constexpr float QS = 0.08838834764831845f * 1.4426950408889634f;
            if (g == 0) st32(rs, W_QA + hq * 2048 + dh * 1024 + (256 * ch + 32 * w + 2 * i16) * 2, san(pk(r0 * QS, r1 * QS)));
            STAMP(2);
        }
        {
            const GAS unsigned char* src = (const GAS unsigned char*)p.ckv + ((size_t)kc * 128 + 16 * w) * 1024 + l * 16;
#pragma unroll
            for (int r = 0; r < 16; ++r)
                __builtin_amdgcn_global_load_lds((const GAS unsigned*)(src + r * 1024), (LAS unsigned*)(L + L_CT + (16 * w + r) * RS), 16, 0, 0);
        }
        STAMP(3);
        BAR();
        STAMP(4);
        v4u qf[16];
        {
            const LAS unsigned char* qp = L + L_QA + i16 * RS + g * 256;
#pragma unroll
            for (int s = 0; s < 16; ++s) qf[s] = *(const LAS v4u*)(qp + s * 16);
        }
        asm volatile("s_waitcnt vmcnt(0)" ::: "memory");
        STAMP(5);
        f32x4 sc = {0.f, 0.f, 0.f, 0.f};
        {
            const int rho = 8 * (i16 >> 3) + 2 * (i16 & 3) + ((i16 >> 2) & 1);
            const LAS unsigned char* cp = L + L_CT + (16 * w + rho) * RS + g * 256;
#pragma unroll
            for (int s = 0; s < 16; ++s) sc = mfma(*(const LAS v4u*)(cp + s * 16), qf[s], sc);
        }
        float mw = fmaxf(fmaxf(sc[0], sc[1]), fmaxf(sc[2], sc[3]));
        mw = x32_max(x16_max(mw));
        if (g == 0) *(LAS float*)(L + L_MX + i16 * 32 + w * 4) = mw;
        STAMP(6);
        BAR();
        v4u cf[4][4];
        {
            const int q4 = i16 >> 2, p4 = i16 & 3;
            const LAS unsigned char* tp = L + L_CT + (8 * g + 2 * q4) * RS + 128 * w + 8 * p4;
            const f32x4 m0 = *(const LAS f32x4*)(L + L_MX + i16 * 32), m1 = *(const LAS f32x4*)(L + L_MX + i16 * 32 + 16);
            mw = fmaxf(fmaxf(fmaxf(m0[0], m0[1]), fmaxf(m0[2], m0[3])), fmaxf(fmaxf(m1[0], m1[1]), fmaxf(m1[2], m1[3])));
            v2u pp = {pk(__builtin_amdgcn_exp2f(sc[0] - mw), __builtin_amdgcn_exp2f(sc[1] - mw)), pk(__builtin_amdgcn_exp2f(sc[2] - mw), __builtin_amdgcn_exp2f(sc[3] - mw))};
            *(LAS v2u*)(L + L_P + i16 * PS + (16 * w + 4 * g) * 2) = pp;
            float ls = (bflo(pp[0]) + bfhi(pp[0])) + (bflo(pp[1]) + bfhi(pp[1]));
            ls = x32_sum(x16_sum(ls));
            if (g == 0) *(LAS float*)(L + L_LS + i16 * 32 + w * 4) = ls;
            BAR();
#pragma unroll
            for (int ks = 0; ks < 4; ++ks)
#pragma unroll
                for (int ct = 0; ct < 4; ++ct) {
                    const s16x4 lo = __builtin_amdgcn_ds_read_tr16_b64_v4i16((LAS s16x4*)(tp + (32 * ks) * RS + 32 * ct));
                    const s16x4 hi = __builtin_amdgcn_ds_read_tr16_b64_v4i16((LAS s16x4*)(tp + (32 * ks + 1) * RS + 32 * ct));
                    const v2u a = __builtin_bit_cast(v2u, lo), c = __builtin_bit_cast(v2u, hi);
                    cf[ks][ct] = (v4u){a[0], a[1], c[0], c[1]};
                }
        }
        STAMP(7);
        f32x4 o[4];
#pragma unroll
        for (int ct = 0; ct < 4; ++ct) o[ct] = (f32x4){0.f, 0.f, 0.f, 0.f};
#pragma unroll
        for (int ks = 0; ks < 4; ++ks) {
            const v4u pf = *(const LAS v4u*)(L + L_P + i16 * PS + ks * 64 + g * 16);
#pragma unroll
            for (int ct = 0; ct < 4; ++ct) o[ct] = mfma(cf[ks][ct], pf, o[ct]);
        }
        const f32x4 l0 = *(const LAS f32x4*)(L + L_LS + i16 * 32), l1 = *(const LAS f32x4*)(L + L_LS + i16 * 32 + 16);
        const float lt = sum4(l0) + sum4(l1);
        const float rl = __builtin_amdgcn_rcpf(lt);
        if (w == 0 && g == 0) st32(rs, W_LS + (hh * 128 + kc) * 128 + i16 * 4, san(__float_as_uint(mw + __builtin_amdgcn_logf(lt))));
        const int ob = W_OB + ((16 * hh + i16) * 8 + w) * 16384 + kc * 128 + g * 16;
#pragma unroll
        for (int hf = 0; hf < 2; ++hf) {
            const f32x4 a = o[2 * hf], c = o[2 * hf + 1];
            v4u v = {san(pk(a[0] * rl, a[1] * rl)), san(pk(a[2] * rl, a[3] * rl)), san(pk(c[0] * rl, c[1] * rl)), san(pk(c[2] * rl, c[3] * rl))};
            st128(rs, ob + hf * 64, v);
        }
        STAMP(8);
        STAMP_OUT();
        return;
    }

    const int cw = w - 8;
    v2u ep = ld64(rs, W_EP);
    if (cw == 0) {
        const v4u ones = {SENT, SENT, SENT, SENT};
        if (isW) {
            const int hq = x * 4 + (pi >> 2), dh = (pi >> 1) & 1, ch = pi & 1;
            if (l < 32) st128(rs, W_QA + hq * 2048 + dh * 1024 + ch * 512 + l * 16, ones);
        } else {
#pragma unroll
            for (int n = 0; n < 3; ++n) st128(rs, W_LS + kc * 3072 + n * 1024 + l * 16, ones);
#pragma unroll 4
            for (int n = 0; n < 32; ++n) st128(rs, W_OB + kc * 32768 + n * 1024 + l * 16, ones);
        }
    }
    u64 tag64;
    {
        const u64 e = ((u64)(unsigned)__builtin_amdgcn_readfirstlane(ep[1]) << 32) | (unsigned)__builtin_amdgcn_readfirstlane(ep[0]);
        tag64 = e * 6364136223846793005ull + 1442695040888963407ull;
    }
    const v2u tag = {(unsigned)tag64, (unsigned)(tag64 >> 32)};
    if (cw == 0) {
        asm volatile("s_waitcnt vmcnt(0)" ::: "memory");
        if (l == 0) st64(rs, W_FL + (isW * 128 + kc) * 128, tag);
    }
    STAMP(1);
    if (isW) BAR();
    {
        const int slot = l < 32 ? 32 * cw + l : 128 + (16 * hh + 4 * cw) * 4 + (l & 15);
        unsigned spins = 0;
        while (true) {
            const v2u f = ld64(rs, W_FL + slot * 128);
            if (__all((f[0] == tag[0]) & (f[1] == tag[1])) || ++spins > SPIN_CAP) break;
            __builtin_amdgcn_s_sleep(8);
            FENCE();
        }
    }
    STAMP(2);
    {
        const int qb = W_QA + (16 * hh + 4 * cw) * 2048;
        unsigned spins = 0;
        while (true) {
            const unsigned f = ld32(rs, qb + (l & 15) * 512 + 508);
            if (__all(f != SENT) || ++spins > SPIN_CAP) break;
            __builtin_amdgcn_s_sleep(4);
            FENCE();
        }
        STAMP(3);
        v4u q0[4], q1[4];
        while (true) {
#pragma unroll
            for (int n = 0; n < 4; ++n) { q0[n] = ld128(rs, qb + n * 2048 + l * 16); q1[n] = ld128(rs, qb + n * 2048 + 1024 + l * 16); }
            unsigned mx = 0;
#pragma unroll
            for (int n = 0; n < 4; ++n) mx = max(mx, max(umax4(q0[n]), umax4(q1[n])));
            if (__all(mx != SENT) || ++spins > SPIN_CAP) break;
            __builtin_amdgcn_s_sleep(2);
            FENCE();
        }
#pragma unroll
        for (int n = 0; n < 4; ++n) {
            v4u o;
#pragma unroll
            for (int t = 0; t < 4; ++t) o[t] = pk(bflo(q0[n][t]) + bflo(q1[n][t]), bfhi(q0[n][t]) + bfhi(q1[n][t]));
            *(LAS v4u*)(L + L_QA + (4 * cw + n) * RS + l * 16) = o;
        }
    }
    STAMP(4);
    BAR();
    const int vh = 4 * x + (i >> 3), vs = i & 7;
    unsigned uvr[2][8];
    {
        const GAS u16* up = (const GAS u16*)p.wuv + (size_t)(64 * vs + 4 * g) * 4096 + vh * 128 + 32 * cw + 2 * i16;
#pragma unroll
        for (int ks = 0; ks < 2; ++ks)
#pragma unroll
            for (int j = 0; j < 8; ++j) uvr[ks][j] = *(const GAS unsigned*)(up + (size_t)(32 * ks + 16 * (j >> 2) + (j & 3)) * 4096);
    }
    __builtin_amdgcn_sched_barrier(0);
    BAR();
    BAR();
    STAMP(5);
    v4u uvf[2][2];
#pragma unroll
    for (int ks = 0; ks < 2; ++ks)
#pragma unroll
        for (int t = 0; t < 4; ++t) { uvf[0][ks][t] = perm_lo(uvr[ks][2 * t], uvr[ks][2 * t + 1]); uvf[1][ks][t] = perm_hi(uvr[ks][2 * t], uvr[ks][2 * t + 1]); }
    const bool isF = !isW && pi < 7;
    const int fc = 7 * x + pi;
    unsigned wr_[4][8];
    v4u w2r[8];
    unsigned xb = 0, b1v = 0; v2u b2v = {0, 0};
    if (isW) {
        const GAS u16* op = (const GAS u16*)p.wo + (size_t)((4 * x + cw) * 128 + 8 * g) * 512 + 32 * pi + 2 * i16;
#pragma unroll
        for (int ks = 0; ks < 4; ++ks)
#pragma unroll
            for (int j = 0; j < 8; ++j) wr_[ks][j] = *(const GAS unsigned*)(op + (32 * ks + j) * 512);
    } else if (isF) {
        const GAS u16* fp = (const GAS u16*)p.w1 + (size_t)(128 * cw + 8 * g) * 1792 + 32 * fc + 2 * i16;
#pragma unroll
        for (int ks = 0; ks < 4; ++ks)
#pragma unroll
            for (int j = 0; j < 8; ++j) wr_[ks][j] = *(const GAS unsigned*)(fp + (32 * ks + j) * 1792);
        const GAS u16* gp = (const GAS u16*)p.w2 + (size_t)(32 * fc + 8 * g) * 512 + 128 * cw + 8 * i16;
#pragma unroll
        for (int j = 0; j < 8; ++j) w2r[j] = *(const GAS v4u*)(gp + j * 512);
        xb = *(const GAS unsigned*)(p.x + 128 * cw + 2 * l);
        b1v = p.b1[32 * fc + 8 * g + (i16 & 7)];
        b2v = *(const GAS v2u*)(p.b2 + 32 * (2 * x + (pi & 1)) + 4 * (l & 7));
    }
    __builtin_amdgcn_sched_barrier(0);
    __builtin_amdgcn_s_sleep(DLY_B);
    {
        const int lsb = W_LS + (vh >> 4) * 128 * 128 + (vh & 15) * 4;
        const int obb = W_OB + (vh * 8 + vs) * 16384 + cw * 4096 + l * 16;
        unsigned la0, la1, lo_[4];
        v4u od[4];
        unsigned spins = 0;
        while (true) {
            la0 = ld32(rs, lsb + l * 128); la1 = ld32(rs, lsb + (64 + l) * 128);
#pragma unroll
            for (int n = 0; n < 4; ++n) lo_[n] = ld32(rs, lsb + (32 * cw + 8 * n + (l >> 3)) * 128);
#pragma unroll
            for (int n = 0; n < 4; ++n) od[n] = ld128(rs, obb + n * 1024);
            unsigned mx = max(la0, la1);
#pragma unroll
            for (int n = 0; n < 4; ++n) mx = max(mx, max(lo_[n], umax4(od[n])));
            if (__all(mx != SENT) || ++spins > SPIN_CAP) break;
            __builtin_amdgcn_s_sleep(1);
            FENCE();
        }
        STAMP(6);
        const float M = wave_max(fmaxf(__uint_as_float(la0), __uint_as_float(la1)));
        const unsigned ew = pk(__builtin_amdgcn_exp2f(__uint_as_float(la0) - M), __builtin_amdgcn_exp2f(__uint_as_float(la1) - M));
        const float Lr = __builtin_amdgcn_rcpf(wave_sum(bflo(ew) + bfhi(ew)));
        f32x4 acc = {0.f, 0.f, 0.f, 0.f};
#pragma unroll
        for (int n = 0; n < 4; ++n) {
            const float e = __builtin_amdgcn_exp2f(__uint_as_float(lo_[n]) - M);
            const unsigned dup = pk(e, e);
            const v4u a = {dup & selm[0], dup & selm[1], dup & selm[2], dup & selm[3]};
            acc = mfma(a, od[n], acc);
        }
        if ((g >> 1) == (i16 >> 3)) {
            const v2u hi = {pk(acc[0], acc[1]), pk(acc[2], acc[3])};
            const v2u lo = {pk(acc[0] - bflo(hi[0]), acc[1] - bfhi(hi[0])), pk(acc[2] - bflo(hi[1]), acc[3] - bfhi(hi[1]))};
            LAS unsigned char* d = L + L_SC + (2 * cw + (g >> 1)) * 144 + (8 * (i16 & 7) + 4 * (g & 1)) * 2;
            *(LAS v2u*)d = hi; *(LAS v2u*)(d + 8 * 144) = lo;
        }
        BAR();
        const v4u a0 = *(const LAS v4u*)(L + L_SC + i16 * 144 + 16 * g), a1 = *(const LAS v4u*)(L + L_SC + i16 * 144 + 64 + 16 * g);
        f32x4 d0 = {0.f, 0.f, 0.f, 0.f}, d1 = {0.f, 0.f, 0.f, 0.f};
        d0 = mfma(a0, uvf[0][0], d0); d1 = mfma(a0, uvf[1][0], d1);
        d0 = mfma(a1, uvf[0][1], d0); d1 = mfma(a1, uvf[1][1], d1);
        float r0 = sum4(d0), r1 = sum4(d1);
        red2_rows(r0, r1);
        r0 *= Lr; r1 *= Lr;
        const unsigned hi = pk(r0, r1), lo = pk(r0 - bflo(hi), r1 - bfhi(hi));
        if (g < 2) st32(rs, W_CX + (vh * 8 + vs) * 512 + cw * 128 + g * 64 + i16 * 4, san(g ? lo : hi));
    }
    STAMP(7);
    if (!isW && !isF) { STAMP_OUT(); return; }
    v4u wf[2][4];
#pragma unroll
    for (int ks = 0; ks < 4; ++ks)
#pragma unroll
        for (int t = 0; t < 4; ++t) { wf[0][ks][t] = perm_lo(wr_[ks][2 * t], wr_[ks][2 * t + 1]); wf[1][ks][t] = perm_hi(wr_[ks][2 * t], wr_[ks][2 * t + 1]); }
    if (isW) {
        __builtin_amdgcn_s_sleep(DLY_V);
        const int cb = W_CX + ((4 * x + cw) * 8 + (i16 & 7)) * 512 + (i16 >> 3) * 64 + g * 16;
        v4u cx[4];
        unsigned spins = 0;
        while (true) {
#pragma unroll
            for (int ks = 0; ks < 4; ++ks) cx[ks] = ld128(rs, cb + ks * 128);
            unsigned mx = 0;
#pragma unroll
            for (int ks = 0; ks < 4; ++ks) mx = max(mx, umax4(cx[ks]));
            if (__all(mx != SENT) || ++spins > SPIN_CAP) break;
            __builtin_amdgcn_s_sleep(1);
            FENCE();
        }
        STAMP(8);
        f32x4 d0 = {0.f, 0.f, 0.f, 0.f}, d1 = {0.f, 0.f, 0.f, 0.f};
#pragma unroll
        for (int ks = 0; ks < 4; ++ks) { d0 = mfma(cx[ks], wf[0][ks], d0); d1 = mfma(cx[ks], wf[1][ks], d1); }
        float r0 = sum4(d0), r1 = sum4(d1);
        red2_rows(r0, r1);
        if (g < 2) *(LAS float*)(L + L_SC + 2560 + (2 * i16 + g) * 16 + cw * 4) = g ? r1 : r0;
        BAR();
        if (cw == 0 && l < 32) {
            const f32x4 v = *(const LAS f32x4*)(L + L_SC + 2560 + l * 16);
            st32(rs, W_OP + x * 2048 + (32 * pi + l) * 4, san(__float_as_uint(sum4(v))));
        }
        STAMP(9);
        STAMP_OUT();
        return;
    }
    v4u w2f[8];
#pragma unroll
    for (int t = 0; t < 4; ++t)
#pragma unroll
        for (int c = 0; c < 4; ++c) { w2f[2 * c][t] = perm_lo(w2r[2 * t][c], w2r[2 * t + 1][c]); w2f[2 * c + 1][t] = perm_hi(w2r[2 * t][c], w2r[2 * t + 1][c]); }
    float cs0, cs1;
    {
        const v4u one = {0x3f803f80u, 0x3f803f80u, 0x3f803f80u, 0x3f803f80u};
        f32x4 d0 = {0.f, 0.f, 0.f, 0.f}, d1 = {0.f, 0.f, 0.f, 0.f};
#pragma unroll
        for (int ks = 0; ks < 4; ++ks) { d0 = mfma(one, wf[0][ks], d0); d1 = mfma(one, wf[1][ks], d1); }
        cs0 = d0[0]; cs1 = d1[0];
    }
    __builtin_amdgcn_s_sleep(DLY_C);
    {
        v2u op[8];
        unsigned spins = 0;
        while (true) {
#pragma unroll
            for (int n = 0; n < 8; ++n) op[n] = ld64(rs, W_OP + n * 2048 + (128 * cw + 2 * l) * 4);
            unsigned mx = 0;
#pragma unroll
            for (int n = 0; n < 8; ++n) mx = max(mx, max(op[n][0], op[n][1]));
            if (__all(mx != SENT) || ++spins > SPIN_CAP) break;
            __builtin_amdgcn_s_sleep(1);
            FENCE();
        }
        STAMP(8);
        float h0 = bflo(xb), h1 = bfhi(xb);
#pragma unroll
        for (int n = 0; n < 8; ++n) { h0 += __uint_as_float(op[n][0]); h1 += __uint_as_float(op[n][1]); }
        const float mw = wave_sum(h0 + h1) * (1.f / 128.f);
        const float c0 = h0 - mw, c1 = h1 - mw;
        const unsigned hi = pk(c0, c1), lo = pk(c0 - bflo(hi), c1 - bfhi(hi));
        *(LAS f32x2*)(L + L_SC + (128 * cw + 2 * l) * 4) = (f32x2){h0, h1};
        LAS unsigned char* hp = L + L_P + cw * 544;
        *(LAS unsigned*)(hp + l * 4) = hi; *(LAS unsigned*)(hp + 272 + l * 4) = lo;
        const float m2 = wave_sum(c0 * c0 + c1 * c1);
        f32x4 d0 = {0.f, 0.f, 0.f, 0.f}, d1 = {0.f, 0.f, 0.f, 0.f};
#pragma unroll
        for (int ks = 0; ks < 4; ++ks) {
            const v4u a = *(const LAS v4u*)(hp + (i16 & 1) * 272 + ks * 64 + g * 16);
            d0 = mfma(a, wf[0][ks], d0); d1 = mfma(a, wf[1][ks], d1);
        }
        if (g == 0) {
            *(LAS float*)(L + L_SC + 2048 + (2 * i16) * 16 + cw * 4) = d0[0] + d0[1];
            *(LAS float*)(L + L_SC + 2048 + (2 * i16 + 1) * 16 + cw * 4) = d1[0] + d1[1];
            *(LAS float*)(L + L_SC + 2560 + (2 * i16) * 16 + cw * 4) = cs0;
            *(LAS float*)(L + L_SC + 2560 + (2 * i16 + 1) * 16 + cw * 4) = cs1;
            if (i16 == 0) { *(LAS float*)(L + L_SC + 3072 + cw * 4) = mw; *(LAS float*)(L + L_SC + 3088 + cw * 4) = m2; }
        }
        BAR();
        const int f = 8 * g + (i16 & 7);
        const f32x4 uu = *(const LAS f32x4*)(L + L_SC + 2048 + f * 16), cc = *(const LAS f32x4*)(L + L_SC + 2560 + f * 16);
        const f32x4 mm = *(const LAS f32x4*)(L + L_SC + 3072), qq = *(const LAS f32x4*)(L + L_SC + 3088);
        const float mean = sum4(mm) * 0.25f;
        const f32x4 dm = mm - mean;
        const float var = (sum4(qq) + 128.f * sum4(dm * dm)) * (1.f / 512.f);
        const float rstd = __builtin_amdgcn_rsqf(var + 1e-5f);
        const float u = (sum4(uu) + sum4(dm * cc)) * rstd + bflo(b1v);
        const float z = u * (1.f + 0.044715f * u * u) * (-2.f * 0.7978845608028654f * 1.4426950408889634f);
        const float ge = u * __builtin_amdgcn_rcpf(1.f + __builtin_amdgcn_exp2f(z));
        const unsigned pr = pk(ge, dpp<0x101>(ge));
        const v4u a = {pr, dppu<0x102>(pr), dppu<0x104>(pr), dppu<0x106>(pr)};
        float y[8];
#pragma unroll
        for (int t = 0; t < 8; ++t) { f32x4 d = {0.f, 0.f, 0.f, 0.f}; d = mfma(a, w2f[t], d); y[t] = d[0]; }
        if ((g & 1) == 0) {
            v4u v;
#pragma unroll
            for (int t = 0; t < 4; ++t) v[t] = san(__float_as_uint(g ? y[4 + t] : y[t]));
            st128(rs, W_YP + ((4 * cw + (i16 >> 2)) * 56 + fc) * 128 + (i16 & 3) * 32 + (g >> 1) * 16, v);
        }
    }
    STAMP(9);
    if (pi < 2 && cw == 0) {
        const int fin = 2 * x + pi;
        __builtin_amdgcn_s_sleep(DLY_D);
        v4u yv[7];
        unsigned spins = 0;
        while (true) {
#pragma unroll
            for (int n = 0; n < 7; ++n) yv[n] = ld128(rs, W_YP + fin * 56 * 128 + n * 1024 + l * 16);
            unsigned mx = 0;
#pragma unroll
            for (int n = 0; n < 7; ++n) mx = max(mx, umax4(yv[n]));
            if (__all(mx != SENT) || ++spins > SPIN_CAP) break;
            __builtin_amdgcn_s_sleep(1);
            FENCE();
        }
        STAMP(10);
        f32x4 s = __builtin_bit_cast(f32x4, yv[0]);
#pragma unroll
        for (int n = 1; n < 7; ++n) s += __builtin_bit_cast(f32x4, yv[n]);
        float s0 = s[0], s1 = s[1], s2 = s[2], s3 = s[3];
        red2_rows(s0, s1); red2_rows(s2, s3);
        s0 += dpp<0x128>(s0); s1 += dpp<0x128>(s1); s2 += dpp<0x128>(s2); s3 += dpp<0x128>(s3);
        const f32x4 hv = *(const LAS f32x4*)(L + L_SC + (32 * fin + 4 * (l & 7)) * 4);
        if (l < 8) {
            const v2u o = {pk(hv[0] + bflo(b2v[0]) + s0, hv[1] + bfhi(b2v[0]) + s1), pk(hv[2] + bflo(b2v[1]) + s2, hv[3] + bfhi(b2v[1]) + s3)};
            *(GAS v2u*)(p.out + 32 * fin + 4 * l) = o;
        }
        if (fin == 0 && l == 0) st64(rs, W_EP, tag);
    }
    STAMP(11);
    STAMP_OUT();
}

extern "C" void kernel_launch(void* const* d_in, const int* in_sizes, int n_in, void* d_out, int out_size, void* d_ws, size_t ws_size,
                              hipStream_t stream) {
    Params p;
    p.x = (const u16*)d_in[0]; p.ckv = (const u16*)d_in[1]; p.wq = (const u16*)d_in[2]; p.wuk = (const u16*)d_in[3];
    p.wuv = (const u16*)d_in[4]; p.wo = (const u16*)d_in[5]; p.w1 = (const u16*)d_in[7]; p.b1 = (const u16*)d_in[8];
    p.w2 = (const u16*)d_in[9]; p.b2 = (const u16*)d_in[10]; p.out = (u16*)d_out; p.ws = (unsigned char*)d_ws;
    (void)hipFuncSetAttribute((const void*)fused_k, hipFuncAttributeMaxDynamicSharedMemorySize, L_END);
    hipLaunchKernelGGL(fused_k, dim3(NWG), dim3(NT), L_END, stream, p);
}
```

```cpp
#include <hip/hip_runtime.h>
#include <stdint.h>

typedef unsigned short u16;
typedef unsigned long long u64;
typedef unsigned v4u __attribute__((ext_vector_type(4)));
typedef unsigned v2u __attribute__((ext_vector_type(2)));
typedef float f32x4 __attribute__((ext_vector_type(4)));
typedef float f32x2 __attribute__((ext_vector_type(2)));
typedef __bf16 bf16x8 __attribute__((ext_vector_type(8)));
typedef __bf16 bf16x2 __attribute__((ext_vector_type(2)));
typedef short s16x4 __attribute__((ext_vector_type(4)));
#define GAS __attribute__((address_space(1)))
#define LAS __attribute__((address_space(3)))
#define DI __device__ __forceinline__

#define STAMPS 1
#define AMP 2
#define DLY_B 24
#define DLY_V 8
#define DLY_C 36
#define DLY_D 8

constexpr int NWG = 256, NT = 768;
constexpr int W_EP = 0, W_FL = 4096, W_QA = 65536, W_LS = W_QA + 65536, W_CX = W_LS + 32768, W_OP = W_CX + 131072,
              W_YP = W_OP + 16384, W_SMALL_END = W_LS + 128 * 3072, W_OB = 1 << 20, W_END = W_OB + (4 << 20), W_ST = 8 << 20;
static_assert(W_YP + 16 * 56 * 128 <= W_SMALL_END && W_SMALL_END <= W_OB, "ws map");
constexpr int RS = 1040, L_CT = 0, L_QA = 128 * RS, L_P = L_QA + 16 * RS, PS = 272, L_MX = L_P + 16 * PS, L_LS = L_MX + 512,
              L_SC = L_LS + 512, L_END = L_SC + 4096;
constexpr unsigned SENT = 0xFFFFFFFFu;
constexpr unsigned SPIN_CAP = 200000;

struct Params {
    const u16 *x, *ckv, *wq, *wuk, *wuv, *wo, *w1, *b1, *w2, *b2;
    u16* out;
    unsigned char* ws;
};

DI float bflo(unsigned u) { return __uint_as_float(u << 16); }
DI float bfhi(unsigned u) { return __uint_as_float(u & 0xffff0000u); }
DI unsigned pk(float a, float b) { f32x2 f = {a, b}; return __builtin_bit_cast(unsigned, __builtin_convertvector(f, bf16x2)); }
DI unsigned san(unsigned u) { return u < 0xFFFFFFFEu ? u : 0xFFFFFFFEu; }
DI f32x4 mfma(v4u a, v4u b, f32x4 c) { return __builtin_amdgcn_mfma_f32_16x16x32_bf16(__builtin_bit_cast(bf16x8, a), __builtin_bit_cast(bf16x8, b), c, 0, 0, 0); }
template <int CTRL> DI float dpp(float v) { return __int_as_float(__builtin_amdgcn_update_dpp(0, __float_as_int(v), CTRL, 0xf, 0xf, true)); }
template <int CTRL> DI unsigned dppu(unsigned v) { return (unsigned)__builtin_amdgcn_update_dpp(0, (int)v, CTRL, 0xf, 0xf, true); }
DI float x16_sum(float v) { auto r = __builtin_amdgcn_permlane16_swap(__float_as_uint(v), __float_as_uint(v), false, false); return __uint_as_float(r[0]) + __uint_as_float(r[1]); }
DI float x32_sum(float v) { auto r = __builtin_amdgcn_permlane32_swap(__float_as_uint(v), __float_as_uint(v), false, false); return __uint_as_float(r[0]) + __uint_as_float(r[1]); }
DI float x16_max(float v) { auto r = __builtin_amdgcn_permlane16_swap(__float_as_uint(v), __float_as_uint(v), false, false); return fmaxf(__uint_as_float(r[0]), __uint_as_float(r[1])); }
DI float x32_max(float v) { auto r = __builtin_amdgcn_permlane32_swap(__float_as_uint(v), __float_as_uint(v), false, false); return fmaxf(__uint_as_float(r[0]), __uint_as_float(r[1])); }
DI float wave_sum(float v) {
    v += dpp<0xB1>(v); v += dpp<0x4E>(v); v += dpp<0x141>(v); v += dpp<0x140>(v);
    return x32_sum(x16_sum(v));
}
DI float wave_max(float v) {
    v = fmaxf(v, dpp<0xB1>(v)); v = fmaxf(v, dpp<0x4E>(v)); v = fmaxf(v, dpp<0x141>(v)); v = fmaxf(v, dpp<0x140>(v));
    return x32_max(x16_max(v));
}
DI void red2_rows(float& a, float& b) {
    auto r = __builtin_amdgcn_permlane32_swap(__float_as_uint(a), __float_as_uint(b), false, false);
    float s = __uint_as_float(r[0]) + __uint_as_float(r[1]);
    s = x16_sum(s);
    auto q = __builtin_amdgcn_permlane32_swap(__float_as_uint(s), __float_as_uint(s), false, false);
    a = __uint_as_float(q[0]); b = __uint_as_float(q[1]);
}
DI float sum4(f32x4 v) { return (v[0] + v[1]) + (v[2] + v[3]); }
DI unsigned umax4(v4u v) { return max(max(v[0], v[1]), max(v[2], v[3])); }
#define BAR() do { asm volatile("s_waitcnt lgkmcnt(0)" ::: "memory"); __builtin_amdgcn_s_barrier(); asm volatile("" ::: "memory"); } while (0)
#define FENCE() asm volatile("" ::: "memory")
typedef __amdgpu_buffer_rsrc_t rsrc_t;
DI v4u ld128(rsrc_t r, int off) { return __builtin_amdgcn_raw_buffer_load_b128(r, off, 0, 16); }
DI v2u ld64(rsrc_t r, int off) { return __builtin_amdgcn_raw_buffer_load_b64(r, off, 0, 16); }
DI unsigned ld32(rsrc_t r, int off) { return __builtin_amdgcn_raw_buffer_load_b32(r, off, 0, 16); }
DI void st128(rsrc_t r, int off, v4u v) { __builtin_amdgcn_raw_buffer_store_b128(v, r, off, 0, 16); }
DI void st64(rsrc_t r, int off, v2u v) { __builtin_amdgcn_raw_buffer_store_b64(v, r, off, 0, 16); }
DI void st32(rsrc_t r, int off, unsigned v) { __builtin_amdgcn_raw_buffer_store_b32(v, r, off, 0, 16); }
DI unsigned perm_lo(unsigned a, unsigned b) { return __builtin_amdgcn_perm(b, a, 0x05040100u); }
DI unsigned perm_hi(unsigned a, unsigned b) { return __builtin_amdgcn_perm(b, a, 0x07060302u); }

#define NST 12
#define STAMP(i) do { __builtin_amdgcn_sched_barrier(0); asm volatile("s_memrealtime %0\n\ts_waitcnt lgkmcnt(0)" : "=s"(tst[i]) :: "memory"); __builtin_amdgcn_sched_barrier(0); } while (0)
#define STAMP_OUT() do { asm volatile("" :: "s"(nsp)); } while (0)

__global__ void __launch_bounds__(NT, 3) fused_k(Params p) {
    extern __shared__ __attribute__((aligned(16))) unsigned char lds_[];
    LAS unsigned char* L = (LAS unsigned char*)lds_;
    const int b = blockIdx.x, tid = threadIdx.x, l = tid & 63, w = __builtin_amdgcn_readfirstlane(tid >> 6);
    const int g = l >> 4, i16 = l & 15;
    const int x = b & 7, i = b >> 3, isW = i & 1, pi = i >> 1, kc = x * 16 + pi, hh = isW;
    const rsrc_t rs = __builtin_amdgcn_make_buffer_rsrc(p.ws, 0, W_END, 0x00020000);
    u64 tst[NST]; unsigned nsp = 0;
#pragma unroll
    for (int i_ = 0; i_ < NST; ++i_) tst[i_] = 0;
    STAMP(0);
    v4u selm;
#pragma unroll
    for (int t = 0; t < 4; ++t) selm[t] = (((l & 7) >> 1) == t) ? ((l & 1) ? 0xffff0000u : 0x0000ffffu) : 0u;

    if (w < 8) {
        if (isW) {
            const int hq = x * 4 + (pi >> 2), dh = (pi >> 1) & 1, ch = pi & 1;
            const int r8 = l >> 3, p8 = l & 7, wr = w ^ (4 * ch);
            const v4u xs = *(const GAS v4u*)(p.x + 8 * l);
            const v4u xr = *(const GAS v4u*)(p.x + 64 * wr + 8 * r8);
            v4u wq[8], uk[2][2];
            const GAS u16* wqp = (const GAS u16*)p.wq + (size_t)(64 * wr + 8 * r8) * 4096 + hq * 128 + dh * 64 + 8 * p8;
#pragma unroll
            for (int n = 0; n < 8; ++n) wq[n] = *(const GAS v4u*)(wqp + n * 4096);
            const GAS u16* ukp = (const GAS u16*)p.wuk + (size_t)(256 * ch + 32 * w + 2 * i16) * 4096 + hq * 128 + dh * 64 + 8 * g;
#pragma unroll
            for (int tt = 0; tt < 2; ++tt)
#pragma unroll
                for (int s = 0; s < 2; ++s) uk[tt][s] = *(const GAS v4u*)(ukp + tt * 4096 + 32 * s);
            __builtin_amdgcn_sched_barrier(0);
            float xv[8];
#pragma unroll
            for (int t = 0; t < 4; ++t) { xv[2 * t] = bflo(xs[t]); xv[2 * t + 1] = bfhi(xs[t]); }
            float sm = 0.f;
#pragma unroll
            for (int t = 0; t < 8; ++t) sm += xv[t];
            const float mean = wave_sum(sm) * (1.f / 512.f);
            float sq = 0.f;
#pragma unroll
            for (int t = 0; t < 8; ++t) { const float d = xv[t] - mean; sq += d * d; }
            const float rstd = __builtin_amdgcn_rsqf(wave_sum(sq) * (1.f / 512.f) + 1e-5f);
            f32x4 acc = {0.f, 0.f, 0.f, 0.f};
#pragma unroll
            for (int n = 0; n < 8; ++n) {
                const float xn = (((n & 1) ? bfhi(xr[n >> 1]) : bflo(xr[n >> 1])) - mean) * rstd;
                const unsigned dup = pk(xn, xn);
                const v4u a = {dup & selm[0], dup & selm[1], dup & selm[2], dup & selm[3]};
                acc = mfma(a, wq[n], acc);
            }
            if ((g >> 1) == (i16 >> 3)) {
                v2u o = {pk(acc[0], acc[1]), pk(acc[2], acc[3])};
                *(LAS v2u*)(L + L_SC + (2 * w + (g >> 1)) * 144 + (8 * (i16 & 7) + 4 * (g & 1)) * 2) = o;
            }
            STAMP(1);
            BAR();
            const v4u a0 = *(const LAS v4u*)(L + L_SC + i16 * 144 + 16 * g), a1 = *(const LAS v4u*)(L + L_SC + i16 * 144 + 64 + 16 * g);
            f32x4 d0 = {0.f, 0.f, 0.f, 0.f}, d1 = {0.f, 0.f, 0.f, 0.f};
            d0 = mfma(a0, uk[0][0], d0); d1 = mfma(a0, uk[1][0], d1);
            d0 = mfma(a1, uk[0][1], d0); d1 = mfma(a1, uk[1][1], d1);
            float r0 = sum4(d0), r1 = sum4(d1);
            red2_rows(r0, r1);
            constexpr float QS = 0.08838834764831845f * 1.4426950408889634f;
            if (g == 0) st32(rs, W_QA + hq * 2048 + dh * 1024 + (256 * ch + 32 * w + 2 * i16) * 2, san(pk(r0 * QS, r1 * QS)));
            STAMP(2);
        }
        {
            const GAS unsigned char* src = (const GAS unsigned char*)p.ckv + ((size_t)kc * 128 + 16 * w) * 1024 + l * 16;
#pragma unroll
            for (int r = 0; r < 16; ++r)
                __builtin_amdgcn_global_load_lds((const GAS unsigned*)(src + r * 1024), (LAS unsigned*)(L + L_CT + (16 * w + r) * RS), 16, 0, 0);
        }
        STAMP(3);
        BAR();
        STAMP(4);
        v4u qf[16];
        {
            const LAS unsigned char* qp = L + L_QA + i16 * RS + g * 256;
#pragma unroll
            for (int s = 0; s < 16; ++s) qf[s] = *(const LAS v4u*)(qp + s * 16);
        }
        asm volatile("s_waitcnt vmcnt(0)" ::: "memory");
        STAMP(5);
        f32x4 sc = {0.f, 0.f, 0.f, 0.f};
        {
            const int rho = 8 * (i16 >> 3) + 2 * (i16 & 3) + ((i16 >> 2) & 1);
            const LAS unsigned char* cp = L + L_CT + (16 * w + rho) * RS + g * 256;
#pragma unroll
            for (int s = 0; s < 16; ++s) sc = mfma(*(const LAS v4u*)(cp + s * 16), qf[s], sc);
        }
        float mw = fmaxf(fmaxf(sc[0], sc[1]), fmaxf(sc[2], sc[3]));
        mw = x32_max(x16_max(mw));
        if (g == 0) *(LAS float*)(L + L_MX + i16 * 32 + w * 4) = mw;
        STAMP(6);
        BAR();
        v4u cf[4][4];
        {
            const int q4 = i16 >> 2, p4 = i16 & 3;
            const LAS unsigned char* tp = L + L_CT + (8 * g + 2 * q4) * RS + 128 * w + 8 * p4;
            const f32x4 m0 = *(const LAS f32x4*)(L + L_MX + i16 * 32), m1 = *(const LAS f32x4*)(L + L_MX + i16 * 32 + 16);
            mw = fmaxf(fmaxf(fmaxf(m0[0], m0[1]), fmaxf(m0[2], m0[3])), fmaxf(fmaxf(m1[0], m1[1]), fmaxf(m1[2], m1[3])));
            v2u pp = {pk(__builtin_amdgcn_exp2f(sc[0] - mw), __builtin_amdgcn_exp2f(sc[1] - mw)), pk(__builtin_amdgcn_exp2f(sc[2] - mw), __builtin_amdgcn_exp2f(sc[3] - mw))};
            *(LAS v2u*)(L + L_P + i16 * PS + (16 * w + 4 * g) * 2) = pp;
            float ls = (bflo(pp[0]) + bfhi(pp[0])) + (bflo(pp[1]) + bfhi(pp[1]));
            ls = x32_sum(x16_sum(ls));
            if (g == 0) *(LAS float*)(L + L_LS + i16 * 32 + w * 4) = ls;
            BAR();
#pragma unroll
            for (int ks = 0; ks < 4; ++ks)
#pragma unroll
                for (int ct = 0; ct < 4; ++ct) {
                    const s16x4 lo = __builtin_amdgcn_ds_read_tr16_b64_v4i16((LAS s16x4*)(tp + (32 * ks) * RS + 32 * ct));
                    const s16x4 hi = __builtin_amdgcn_ds_read_tr16_b64_v4i16((LAS s16x4*)(tp + (32 * ks + 1) * RS + 32 * ct));
                    const v2u a = __builtin_bit_cast(v2u, lo), c = __builtin_bit_cast(v2u, hi);
                    cf[ks][ct] = (v4u){a[0], a[1], c[0], c[1]};
                }
        }
        STAMP(7);
        f32x4 o[4];
#pragma unroll
        for (int ct = 0; ct < 4; ++ct) o[ct] = (f32x4){0.f, 0.f, 0.f, 0.f};
#pragma unroll
        for (int ks = 0; ks < 4; ++ks) {
            const v4u pf = *(const LAS v4u*)(L + L_P + i16 * PS + ks * 64 + g * 16);
#pragma unroll
            for (int ct = 0; ct < 4; ++ct) o[ct] = mfma(cf[ks][ct], pf, o[ct]);
        }
        const f32x4 l0 = *(const LAS f32x4*)(L + L_LS + i16 * 32), l1 = *(const LAS f32x4*)(L + L_LS + i16 * 32 + 16);
        const float lt = sum4(l0) + sum4(l1);
        const float rl = __builtin_amdgcn_rcpf(lt);
        if (w == 0 && g == 0) st32(rs, W_LS + (hh * 128 + kc) * 128 + i16 * 4, san(__float_as_uint(mw + __builtin_amdgcn_logf(lt))));
        const int ob = W_OB + ((16 * hh + i16) * 8 + w) * 16384 + kc * 128 + g * 16;
#pragma unroll
        for (int hf = 0; hf < 2; ++hf) {
            const f32x4 a = o[2 * hf], c = o[2 * hf + 1];
            v4u v = {san(pk(a[0] * rl, a[1] * rl)), san(pk(a[2] * rl, a[3] * rl)), san(pk(c[0] * rl, c[1] * rl)), san(pk(c[2] * rl, c[3] * rl))};
            st128(rs, ob + hf * 64, v);
        }
        STAMP(8);
        STAMP_OUT();
        return;
    }

    const int cw = w - 8;
    v2u ep = ld64(rs, W_EP);
    if (cw == 0) {
        const v4u ones = {SENT, SENT, SENT, SENT};
        if (isW) {
            const int hq = x * 4 + (pi >> 2), dh = (pi >> 1) & 1, ch = pi & 1;
            if (l < 32) st128(rs, W_QA + hq * 2048 + dh * 1024 + ch * 512 + l * 16, ones);
        } else {
#pragma unroll
            for (int n = 0; n < 3; ++n) st128(rs, W_LS + kc * 3072 + n * 1024 + l * 16, ones);
#pragma unroll 4
            for (int n = 0; n < 32; ++n) st128(rs, W_OB + kc * 32768 + n * 1024 + l * 16, ones);
        }
    }
    u64 tag64;
    {
        const u64 e = ((u64)(unsigned)__builtin_amdgcn_readfirstlane(ep[1]) << 32) | (unsigned)__builtin_amdgcn_readfirstlane(ep[0]);
        tag64 = e * 6364136223846793005ull + 1442695040888963407ull;
    }
    const v2u tag = {(unsigned)tag64, (unsigned)(tag64 >> 32)};
    if (cw == 0) {
        asm volatile("s_waitcnt vmcnt(0)" ::: "memory");
        if (l == 0) st64(rs, W_FL + (isW * 128 + kc) * 128, tag);
    }
    STAMP(1);
    if (isW) BAR();
    {
        const int slot = l < 32 ? 32 * cw + l : 128 + (16 * hh + 4 * cw) * 4 + (l & 15);
        unsigned spins = 0;
        while (true) {
            const v2u f = ld64(rs, W_FL + slot * 128);
            if (__all((f[0] == tag[0]) & (f[1] == tag[1])) || ++spins > SPIN_CAP) break;
            __builtin_amdgcn_s_sleep(8);
            FENCE();
        }
    }
    STAMP(2);
    {
        const int qb = W_QA + (16 * hh + 4 * cw) * 2048;
        unsigned spins = 0;
        while (true) {
            const unsigned f = ld32(rs, qb + (l & 15) * 512 + 508);
            if (__all(f != SENT) || ++spins > SPIN_CAP) break;
            __builtin_amdgcn_s_sleep(4);
            FENCE();
        }
        STAMP(3);
        v4u q0[4], q1[4];
        while (true) {
#pragma unroll
            for (int n = 0; n < 4; ++n) { q0[n] = ld128(rs, qb + n * 2048 + l * 16); q1[n] = ld128(rs, qb + n * 2048 + 1024 + l * 16); }
            unsigned mx = 0;
#pragma unroll
            for (int n = 0; n < 4; ++n) mx = max(mx, max(umax4(q0[n]), umax4(q1[n])));
            if (__all(mx != SENT) || ++spins > SPIN_CAP) break;
            __builtin_amdgcn_s_sleep(2);
            FENCE();
        }
#pragma unroll
        for (int n = 0; n < 4; ++n) {
            v4u o;
#pragma unroll
            for (int t = 0; t < 4; ++t) o[t] = pk(bflo(q0[n][t]) + bflo(q1[n][t]), bfhi(q0[n][t]) + bfhi(q1[n][t]));
            *(LAS v4u*)(L + L_QA + (4 * cw + n) * RS + l * 16) = o;
        }
    }
    STAMP(4);
    BAR();
    const int vh = 4 * x + (i >> 3), vs = i & 7;
    unsigned uvr[2][8];
    {
        const GAS u16* up = (const GAS u16*)p.wuv + (size_t)(64 * vs + 4 * g) * 4096 + vh * 128 + 32 * cw + 2 * i16;
#pragma unroll
        for (int ks = 0; ks < 2; ++ks)
#pragma unroll
            for (int j = 0; j < 8; ++j) uvr[ks][j] = *(const GAS unsigned*)(up + (size_t)(32 * ks + 16 * (j >> 2) + (j & 3)) * 4096);
    }
    __builtin_amdgcn_sched_barrier(0);
    BAR();
    BAR();
    STAMP(5);
    v4u uvf[2][2];
#pragma unroll
    for (int ks = 0; ks < 2; ++ks)
#pragma unroll
        for (int t = 0; t < 4; ++t) { uvf[0][ks][t] = perm_lo(uvr[ks][2 * t], uvr[ks][2 * t + 1]); uvf[1][ks][t] = perm_hi(uvr[ks][2 * t], uvr[ks][2 * t + 1]); }
    const bool isF = !isW && pi < 7;
    const int fc = 7 * x + pi;
    unsigned wr_[4][8];
    v4u w2r[8];
    unsigned xb = 0, b1v = 0; v2u b2v = {0, 0};
    if (isW) {
        const GAS u16* op = (const GAS u16*)p.wo + (size_t)((4 * x + cw) * 128 + 8 * g) * 512 + 32 * pi + 2 * i16;
#pragma unroll
        for (int ks = 0; ks < 4; ++ks)
#pragma unroll
            for (int j = 0; j < 8; ++j) wr_[ks][j] = *(const GAS unsigned*)(op + (32 * ks + j) * 512);
    } else if (isF) {
        const GAS u16* fp = (const GAS u16*)p.w1 + (size_t)(128 * cw + 8 * g) * 1792 + 32 * fc + 2 * i16;
#pragma unroll
        for (int ks = 0; ks < 4; ++ks)
#pragma unroll
            for (int j = 0; j < 8; ++j) wr_[ks][j] = *(const GAS unsigned*)(fp + (32 * ks + j) * 1792);
        const GAS u16* gp = (const GAS u16*)p.w2 + (size_t)(32 * fc + 8 * g) * 512 + 128 * cw + 8 * i16;
#pragma unroll
        for (int j = 0; j < 8; ++j) w2r[j] = *(const GAS v4u*)(gp + j * 512);
        xb = *(const GAS unsigned*)(p.x + 128 * cw + 2 * l);
        b1v = p.b1[32 * fc + 8 * g + (i16 & 7)];
        b2v = *(const GAS v2u*)(p.b2 + 32 * (2 * x + (pi & 1)) + 4 * (l & 7));
    }
    __builtin_amdgcn_sched_barrier(0);
    __builtin_amdgcn_s_sleep(DLY_B);
    {
        const int lsb = W_LS + (vh >> 4) * 128 * 128 + (vh & 15) * 4;
        const int obb = W_OB + (vh * 8 + vs) * 16384 + cw * 4096 + l * 16;
        unsigned la0, la1, lo_[4];
        v4u od[4];
        unsigned spins = 0;
        while (true) {
            la0 = ld32(rs, lsb + l * 128); la1 = ld32(rs, lsb + (64 + l) * 128);
#pragma unroll
            for (int n = 0; n < 4; ++n) lo_[n] = ld32(rs, lsb + (32 * cw + 8 * n + (l >> 3)) * 128);
#pragma unroll
            for (int n = 0; n < 4; ++n) od[n] = ld128(rs, obb + n * 1024);
            unsigned mx = max(la0, la1);
#pragma unroll
            for (int n = 0; n < 4; ++n) mx = max(mx, max(lo_[n], umax4(od[n])));
            if (__all(mx != SENT) || ++spins > SPIN_CAP) break;
            __builtin_amdgcn_s_sleep(1);
            FENCE();
        }
        nsp += spins;
        STAMP(6);
        const float M = wave_max(fmaxf(__uint_as_float(la0), __uint_as_float(la1)));
        const unsigned ew = pk(__builtin_amdgcn_exp2f(__uint_as_float(la0) - M), __builtin_amdgcn_exp2f(__uint_as_float(la1) - M));
        const float Lr = __builtin_amdgcn_rcpf(wave_sum(bflo(ew) + bfhi(ew)));
        f32x4 acc = {0.f, 0.f, 0.f, 0.f};
#pragma unroll
        for (int n = 0; n < 4; ++n) {
            const float e = __builtin_amdgcn_exp2f(__uint_as_float(lo_[n]) - M);
            const unsigned dup = pk(e, e);
            const v4u a = {dup & selm[0], dup & selm[1], dup & selm[2], dup & selm[3]};
            acc = mfma(a, od[n], acc);
        }
        if ((g >> 1) == (i16 >> 3)) {
            const v2u hi = {pk(acc[0], acc[1]), pk(acc[2], acc[3])};
            const v2u lo = {pk(acc[0] - bflo(hi[0]), acc[1] - bfhi(hi[0])), pk(acc[2] - bflo(hi[1]), acc[3] - bfhi(hi[1]))};
            LAS unsigned char* d = L + L_SC + (2 * cw + (g >> 1)) * 144 + (8 * (i16 & 7) + 4 * (g & 1)) * 2;
            *(LAS v2u*)d = hi; *(LAS v2u*)(d + 8 * 144) = lo;
        }
        BAR();
        const v4u a0 = *(const LAS v4u*)(L + L_SC + i16 * 144 + 16 * g), a1 = *(const LAS v4u*)(L + L_SC + i16 * 144 + 64 + 16 * g);
        f32x4 d0 = {0.f, 0.f, 0.f, 0.f}, d1 = {0.f, 0.f, 0.f, 0.f};
        d0 = mfma(a0, uvf[0][0], d0); d1 = mfma(a0, uvf[1][0], d1);
        d0 = mfma(a1, uvf[0][1], d0); d1 = mfma(a1, uvf[1][1], d1);
        float r0 = sum4(d0), r1 = sum4(d1);
        red2_rows(r0, r1);
        r0 *= Lr; r1 *= Lr;
        const unsigned hi = pk(r0, r1), lo = pk(r0 - bflo(hi), r1 - bfhi(hi));
        if (g < 2) st32(rs, W_CX + (vh * 8 + vs) * 512 + cw * 128 + g * 64 + i16 * 4, san(g ? lo : hi));
    }
    STAMP(7);
    if (!isW && !isF) { STAMP_OUT(); return; }
    v4u wf[2][4];
#pragma unroll
    for (int ks = 0; ks < 4; ++ks)
#pragma unroll
        for (int t = 0; t < 4; ++t) { wf[0][ks][t] = perm_lo(wr_[ks][2 * t], wr_[ks][2 * t + 1]); wf[1][ks][t] = perm_hi(wr_[ks][2 * t], wr_[ks][2 * t + 1]); }
    if (isW) {
        __builtin_amdgcn_s_sleep(DLY_V);
        const int cb = W_CX + ((4 * x + cw) * 8 + (i16 & 7)) * 512 + (i16 >> 3) * 64 + g * 16;
        v4u cx[4];
        unsigned spins = 0;
        while (true) {
#pragma unroll
            for (int ks = 0; ks < 4; ++ks) cx[ks] = ld128(rs, cb + ks * 128);
            unsigned mx = 0;
#pragma unroll
            for (int ks = 0; ks < 4; ++ks) mx = max(mx, umax4(cx[ks]));
            if (__all(mx != SENT) || ++spins > SPIN_CAP) break;
            __builtin_amdgcn_s_sleep(1);
            FENCE();
        }
        nsp += spins << 8;
        STAMP(8);
        f32x4 d0 = {0.f, 0.f, 0.f, 0.f}, d1 = {0.f, 0.f, 0.f, 0.f};
#pragma unroll
        for (int ks = 0; ks < 4; ++ks) { d0 = mfma(cx[ks], wf[0][ks], d0); d1 = mfma(cx[ks], wf[1][ks], d1); }
        float r0 = sum4(d0), r1 = sum4(d1);
        red2_rows(r0, r1);
        if (g < 2) *(LAS float*)(L + L_SC + 2560 + (2 * i16 + g) * 16 + cw * 4) = g ? r1 : r0;
        BAR();
        if (cw == 0 && l < 32) {
            const f32x4 v = *(const LAS f32x4*)(L + L_SC + 2560 + l * 16);
            st32(rs, W_OP + x * 2048 + (32 * pi + l) * 4, san(__float_as_uint(sum4(v))));
        }
        STAMP(9);
        STAMP_OUT();
        return;
    }
    v4u w2f[8];
#pragma unroll
    for (int t = 0; t < 4; ++t)
#pragma unroll
        for (int c = 0; c < 4; ++c) { w2f[2 * c][t] = perm_lo(w2r[2 * t][c], w2r[2 * t + 1][c]); w2f[2 * c + 1][t] = perm_hi(w2r[2 * t][c], w2r[2 * t + 1][c]); }
    float cs0, cs1;
    {
        const v4u one = {0x3f803f80u, 0x3f803f80u, 0x3f803f80u, 0x3f803f80u};
        f32x4 d0 = {0.f, 0.f, 0.f, 0.f}, d1 = {0.f, 0.f, 0.f, 0.f};
#pragma unroll
        for (int ks = 0; ks < 4; ++ks) { d0 = mfma(one, wf[0][ks], d0); d1 = mfma(one, wf[1][ks], d1); }
        cs0 = d0[0]; cs1 = d1[0];
    }
    __builtin_amdgcn_s_sleep(DLY_C);
    {
        v2u op[8];
        unsigned spins = 0;
        while (true) {
#pragma unroll
            for (int n = 0; n < 8; ++n) op[n] = ld64(rs, W_OP + n * 2048 + (128 * cw + 2 * l) * 4);
            unsigned mx = 0;
#pragma unroll
            for (int n = 0; n < 8; ++n) mx = max(mx, max(op[n][0], op[n][1]));
            if (__all(mx != SENT) || ++spins > SPIN_CAP) break;
            __builtin_amdgcn_s_sleep(1);
            FENCE();
        }
        nsp += spins << 8;
        STAMP(8);
        float h0 = bflo(xb), h1 = bfhi(xb);
#pragma unroll
        for (int n = 0; n < 8; ++n) { h0 += __uint_as_float(op[n][0]); h1 += __uint_as_float(op[n][1]); }
        const float mw = wave_sum(h0 + h1) * (1.f / 128.f);
        const float c0 = h0 - mw, c1 = h1 - mw;
        const unsigned hi = pk(c0, c1), lo = pk(c0 - bflo(hi), c1 - bfhi(hi));
        *(LAS f32x2*)(L + L_SC + (128 * cw + 2 * l) * 4) = (f32x2){h0, h1};
        LAS unsigned char* hp = L + L_P + cw * 544;
        *(LAS unsigned*)(hp + l * 4) = hi; *(LAS unsigned*)(hp + 272 + l * 4) = lo;
        const float m2 = wave_sum(c0 * c0 + c1 * c1);
        f32x4 d0 = {0.f, 0.f, 0.f, 0.f}, d1 = {0.f, 0.f, 0.f, 0.f};
#pragma unroll
        for (int ks = 0; ks < 4; ++ks) {
            const v4u a = *(const LAS v4u*)(hp + (i16 & 1) * 272 + ks * 64 + g * 16);
            d0 = mfma(a, wf[0][ks], d0); d1 = mfma(a, wf[1][ks], d1);
        }
        if (g == 0) {
            *(LAS float*)(L + L_SC + 2048 + (2 * i16) * 16 + cw * 4) = d0[0] + d0[1];
            *(LAS float*)(L + L_SC + 2048 + (2 * i16 + 1) * 16 + cw * 4) = d1[0] + d1[1];
            *(LAS float*)(L + L_SC + 2560 + (2 * i16) * 16 + cw * 4) = cs0;
            *(LAS float*)(L + L_SC + 2560 + (2 * i16 + 1) * 16 + cw * 4) = cs1;
            if (i16 == 0) { *(LAS float*)(L + L_SC + 3072 + cw * 4) = mw; *(LAS float*)(L + L_SC + 3088 + cw * 4) = m2; }
        }
        BAR();
        const int f = 8 * g + (i16 & 7);
        const f32x4 uu = *(const LAS f32x4*)(L + L_SC + 2048 + f * 16), cc = *(const LAS f32x4*)(L + L_SC + 2560 + f * 16);
        const f32x4 mm = *(const LAS f32x4*)(L + L_SC + 3072), qq = *(const LAS f32x4*)(L + L_SC + 3088);
        const float mean = sum4(mm) * 0.25f;
        const f32x4 dm = mm - mean;
        const float var = (sum4(qq) + 128.f * sum4(dm * dm)) * (1.f / 512.f);
        const float rstd = __builtin_amdgcn_rsqf(var + 1e-5f);
        const float u = (sum4(uu) + sum4(dm * cc)) * rstd + bflo(b1v);
        const float z = u * (1.f + 0.044715f * u * u) * (-2.f * 0.7978845608028654f * 1.4426950408889634f);
        const float ge = u * __builtin_amdgcn_rcpf(1.f + __builtin_amdgcn_exp2f(z));
        const unsigned pr = pk(ge, dpp<0x101>(ge));
        const v4u a = {pr, dppu<0x102>(pr), dppu<0x104>(pr), dppu<0x106>(pr)};
        float y[8];
#pragma unroll
        for (int t = 0; t < 8; ++t) { f32x4 d = {0.f, 0.f, 0.f, 0.f}; d = mfma(a, w2f[t], d); y[t] = d[0]; }
        if ((g & 1) == 0) {
            v4u v;
#pragma unroll
            for (int t = 0; t < 4; ++t) v[t] = san(__float_as_uint(g ? y[4 + t] : y[t]));
            st128(rs, W_YP + ((4 * cw + (i16 >> 2)) * 56 + fc) * 128 + (i16 & 3) * 32 + (g >> 1) * 16, v);
        }
    }
    STAMP(9);
    if (pi < 2 && cw == 0) {
        const int fin = 2 * x + pi;
        __builtin_amdgcn_s_sleep(DLY_D);
        v4u yv[7];
        unsigned spins = 0;
        while (true) {
#pragma unroll
            for (int n = 0; n < 7; ++n) yv[n] = ld128(rs, W_YP + fin * 56 * 128 + n * 1024 + l * 16);
            unsigned mx = 0;
#pragma unroll
            for (int n = 0; n < 7; ++n) mx = max(mx, umax4(yv[n]));
            if (__all(mx != SENT) || ++spins > SPIN_CAP) break;
            __builtin_amdgcn_s_sleep(1);
            FENCE();
        }
        nsp += spins << 16;
        STAMP(10);
        f32x4 s = __builtin_bit_cast(f32x4, yv[0]);
#pragma unroll
        for (int n = 1; n < 7; ++n) s += __builtin_bit_cast(f32x4, yv[n]);
        float s0 = s[0], s1 = s[1], s2 = s[2], s3 = s[3];
        red2_rows(s0, s1); red2_rows(s2, s3);
        s0 += dpp<0x128>(s0); s1 += dpp<0x128>(s1); s2 += dpp<0x128>(s2); s3 += dpp<0x128>(s3);
        const f32x4 hv = *(const LAS f32x4*)(L + L_SC + (32 * fin + 4 * (l & 7)) * 4);
        if (l < 8) {
            const v2u o = {pk(hv[0] + bflo(b2v[0]) + s0, hv[1] + bfhi(b2v[0]) + s1), pk(hv[2] + bflo(b2v[1]) + s2, hv[3] + bfhi(b2v[1]) + s3)};
            *(GAS v2u*)(p.out + 32 * fin + 4 * l) = o;
        }
        if (fin == 0 && l == 0) st64(rs, W_EP, tag);
    }
    STAMP(11);
    if (pi == 0 && x == 0 && cw == 0) { const u64 until = tst[11] + 10 * (tst[AMP] - tst[0]); while (__builtin_amdgcn_s_memrealtime() < until) __builtin_amdgcn_s_sleep(2); }
    STAMP_OUT();
}

extern "C" void kernel_launch(void* const* d_in, const int* in_sizes, int n_in, void* d_out, int out_size, void* d_ws, size_t ws_size,
                              hipStream_t stream) {
    Params p;
    p.x = (const u16*)d_in[0]; p.ckv = (const u16*)d_in[1]; p.wq = (const u16*)d_in[2]; p.wuk = (const u16*)d_in[3];
    p.wuv = (const u16*)d_in[4]; p.wo = (const u16*)d_in[5]; p.w1 = (const u16*)d_in[7]; p.b1 = (const u16*)d_in[8];
    p.w2 = (const u16*)d_in[9]; p.b2 = (const u16*)d_in[10]; p.out = (u16*)d_out; p.ws = (unsigned char*)d_ws;
    (void)hipFuncSetAttribute((const void*)fused_k, hipFuncAttributeMaxDynamicSharedMemorySize, L_END);
    hipLaunchKernelGGL(fused_k, dim3(NWG), dim3(NT), L_END, stream, p);
}
```

```cpp
#include <hip/hip_runtime.h>
#include <stdint.h>

typedef unsigned short u16;
typedef unsigned long long u64;
typedef unsigned v4u __attribute__((ext_vector_type(4)));
typedef unsigned v2u __attribute__((ext_vector_type(2)));
typedef float f32x4 __attribute__((ext_vector_type(4)));
typedef float f32x2 __attribute__((ext_vector_type(2)));
typedef __bf16 bf16x8 __attribute__((ext_vector_type(8)));
typedef __bf16 bf16x2 __attribute__((ext_vector_type(2)));
typedef short s16x4 __attribute__((ext_vector_type(4)));
#define GAS __attribute__((address_space(1)))
#define LAS __attribute__((address_space(3)))
#define DI __device__ __forceinline__

#define STAMPS 1
#define AMPC 2
#define AMPK 0
#define KNOB(i, v) (v)
#define T_C KNOB(0, 130)
#define T_CLR KNOB(1, 130)
#define T_QP KNOB(2, 170)
#define T_QA KNOB(3, 400)
#define DLY_B KNOB(4, 0)
#define DLY_V KNOB(5, 15)
#define DLY_C KNOB(6, 100)
#define DLY_D KNOB(7, 15)
#define NCLR 8
#define WS_TAIL 1
#define WS_OFF(sz) (WS_TAIL ? (((sz) - (16u << 20)) & ~(size_t)((2u << 20) - 1)) : 0)
#define CODE_KB 19

constexpr int NWG = 256, NT = 768;
constexpr int W_EP = 0, W_FL = 4096, W_QA = 65536, W_LS = W_QA + 65536, W_CX = W_LS + 32768, W_OP = W_CX + 65536,
              W_YP = W_OP + 16384, W_SMALL_END = W_LS + 128 * 2048, W_QP = W_SMALL_END, W_OB = 1 << 20, W_END = W_OB + (4 << 20), W_ST = 8 << 20;
static_assert(W_YP + 16 * 56 * 128 <= W_SMALL_END && W_QP + 262144 <= W_OB, "ws map");
constexpr int RS = 1040, L_CT = 0, L_QA = 128 * RS, L_P = L_QA + 16 * RS, PS = 272, L_MX = L_P + 16 * PS, L_LS = L_MX + 768,
              L_OK = L_LS + 768, L_CN = L_OK + 16, L_SC = L_OK + 64, L_END = L_SC + 4352;
constexpr unsigned SENT = 0xFFFFFFFFu;
constexpr unsigned SPIN_CAP = 200000;

struct Params {
    const u16 *x, *ckv, *wq, *wuk, *wuv, *wo, *w1, *b1, *w2, *b2;
    u16* out;
    unsigned char* ws;
};

DI float bflo(unsigned u) { return __uint_as_float(u << 16); }
DI float bfhi(unsigned u) { return __uint_as_float(u & 0xffff0000u); }
DI unsigned pk(float a, float b) { f32x2 f = {a, b}; return __builtin_bit_cast(unsigned, __builtin_convertvector(f, bf16x2)); }
DI unsigned san(unsigned u) { return u < 0xFFFFFFFEu ? u : 0xFFFFFFFEu; }
DI f32x4 mfma(v4u a, v4u b, f32x4 c) { return __builtin_amdgcn_mfma_f32_16x16x32_bf16(__builtin_bit_cast(bf16x8, a), __builtin_bit_cast(bf16x8, b), c, 0, 0, 0); }
template <int CTRL> DI float dpp(float v) { return __int_as_float(__builtin_amdgcn_update_dpp(0, __float_as_int(v), CTRL, 0xf, 0xf, true)); }
template <int CTRL> DI unsigned dppu(unsigned v) { return (unsigned)__builtin_amdgcn_update_dpp(0, (int)v, CTRL, 0xf, 0xf, true); }
DI float fmx(float a, float b) { return __builtin_elementwise_maximum(a, b); }
DI float fmx3(float a, float b, float c) { return fmx(fmx(a, b), c); }
DI float x16_sum(float v) { auto r = __builtin_amdgcn_permlane16_swap(__float_as_uint(v), __float_as_uint(v), false, false); return __uint_as_float(r[0]) + __uint_as_float(r[1]); }
DI float x32_sum(float v) { auto r = __builtin_amdgcn_permlane32_swap(__float_as_uint(v), __float_as_uint(v), false, false); return __uint_as_float(r[0]) + __uint_as_float(r[1]); }
DI float x16_max(float v) { auto r = __builtin_amdgcn_permlane16_swap(__float_as_uint(v), __float_as_uint(v), false, false); return fmx(__uint_as_float(r[0]), __uint_as_float(r[1])); }
DI float x32_max(float v) { auto r = __builtin_amdgcn_permlane32_swap(__float_as_uint(v), __float_as_uint(v), false, false); return fmx(__uint_as_float(r[0]), __uint_as_float(r[1])); }
DI float wave_sum(float v) {
    v += dpp<0xB1>(v); v += dpp<0x4E>(v); v += dpp<0x141>(v); v += dpp<0x140>(v);
    return x32_sum(x16_sum(v));
}
DI void red2_rows(float& a, float& b) {
    auto r = __builtin_amdgcn_permlane32_swap(__float_as_uint(a), __float_as_uint(b), false, false);
    float s = __uint_as_float(r[0]) + __uint_as_float(r[1]);
    s = x16_sum(s);
    auto q = __builtin_amdgcn_permlane32_swap(__float_as_uint(s), __float_as_uint(s), false, false);
    a = __uint_as_float(q[0]); b = __uint_as_float(q[1]);
}
DI float sum4(f32x4 v) { return (v[0] + v[1]) + (v[2] + v[3]); }
DI float max4(f32x4 v) { return fmx(fmx3(v[0], v[1], v[2]), v[3]); }
DI unsigned umax4(v4u v) { return max(max(v[0], v[1]), max(v[2], v[3])); }
DI f32x4 asf(v4u v) { return __builtin_bit_cast(f32x4, v); }
#define BAR() do { asm volatile("s_waitcnt lgkmcnt(0)" ::: "memory"); __builtin_amdgcn_s_barrier(); asm volatile("" ::: "memory"); } while (0)
#define FENCE() asm volatile("" ::: "memory")
#define SCHED() __builtin_amdgcn_sched_barrier(0)
#define PIN(v) asm volatile("" : "+v"(v))
typedef __amdgpu_buffer_rsrc_t rsrc_t;
DI rsrc_t mkr(const void* p, int bytes) { return __builtin_amdgcn_make_buffer_rsrc((void*)p, 0, bytes, 0x00020000); }
DI v4u ld128(rsrc_t r, int off, int so = 0) { return __builtin_amdgcn_raw_buffer_load_b128(r, off, so, 16); }
DI v2u ld64(rsrc_t r, int off, int so = 0) { return __builtin_amdgcn_raw_buffer_load_b64(r, off, so, 16); }
DI unsigned ld32(rsrc_t r, int off, int so = 0) { return __builtin_amdgcn_raw_buffer_load_b32(r, off, so, 16); }
DI void st128(rsrc_t r, int off, int so, v4u v) {
    __builtin_amdgcn_raw_buffer_store_b128(v, r, off, so, 16);
    asm volatile("s_nop 1" :: "v"(v) : "memory");
}
DI void st64(rsrc_t r, int off, int so, v2u v) { __builtin_amdgcn_raw_buffer_store_b64(v, r, off, so, 16); }
DI void st32(rsrc_t r, int off, int so, unsigned v) { __builtin_amdgcn_raw_buffer_store_b32(v, r, off, so, 16); }
DI v4u in128(rsrc_t r, int off, int so) { return __builtin_amdgcn_raw_buffer_load_b128(r, off, so, 0); }
DI v2u in64(rsrc_t r, int off, int so) { return __builtin_amdgcn_raw_buffer_load_b64(r, off, so, 0); }
DI unsigned in32(rsrc_t r, int off, int so) { return __builtin_amdgcn_raw_buffer_load_b32(r, off, so, 0); }
DI unsigned perm_lo(unsigned a, unsigned b) { return __builtin_amdgcn_perm(b, a, 0x05040100u); }
DI unsigned perm_hi(unsigned a, unsigned b) { return __builtin_amdgcn_perm(b, a, 0x07060302u); }
DI u64 mktag(v2u ep) {
    const u64 e = ((u64)(unsigned)__builtin_amdgcn_readfirstlane(ep[1]) << 32) | (unsigned)__builtin_amdgcn_readfirstlane(ep[0]);
    return e + 0x9E3779B97F4A7C15ull;
}
DI unsigned now32() { return (unsigned)__builtin_amdgcn_s_memrealtime(); }
DI void wait_until(unsigned t0, unsigned ticks) { while (now32() - t0 < ticks) __builtin_amdgcn_s_sleep(1); }
DI v4u l128(unsigned a) { return *(const LAS v4u*)a; }
DI v4u l128v(unsigned a) { return *(const volatile LAS v4u*)a; }
DI void s128(unsigned a, v4u v) { *(LAS v4u*)a = v; }
DI void s64(unsigned a, v2u v) { *(volatile LAS v2u*)a = v; }
DI void s32(unsigned a, unsigned v) { *(volatile LAS unsigned*)a = v; }
DI void s32f(unsigned a, float v) { *(volatile LAS float*)a = v; }
DI v2u ltr(unsigned a) { return __builtin_bit_cast(v2u, __builtin_amdgcn_ds_read_tr16_b64_v4i16((LAS s16x4*)a)); }
typedef unsigned short u16x2 __attribute__((ext_vector_type(2)));
DI unsigned pksubsat(unsigned a, unsigned short d) { return __builtin_bit_cast(unsigned, __builtin_elementwise_sub_sat(__builtin_bit_cast(u16x2, a), (u16x2){d, d})); }
template <int N> struct IC { static constexpr int value = N; };
#define UNLIKELY(c) __builtin_expect(!!(c), 0)
#define POLL(LOADS, BAD, SLEEP, SH) do { LOADS; if (UNLIKELY(BAD)) { unsigned spins = 0; _Pragma("nounroll") do { __builtin_amdgcn_s_sleep(SLEEP); FENCE(); LOADS; ++spins; } while ((BAD) && spins < SPIN_CAP); COUNT(SH); } } while (0)
constexpr int OOB = 0x7ffffff0;
DI v4u sel4(unsigned d, v4u m) { return (v4u){d & m[0], d & m[1], d & m[2], d & m[3]}; }

#define NST 14
#define STAMP(i) do { __builtin_amdgcn_sched_barrier(0); asm volatile("s_memrealtime %0\n\ts_waitcnt lgkmcnt(0)" : "=s"(tst[i]) :: "memory"); __builtin_amdgcn_sched_barrier(0); } while (0)
#define STAMP_OUT() do { if (l == 0) { u64* sp = (u64*)(p.ws + W_ST) + ((size_t)b * 12 + w) * 2; __hip_atomic_store((GAS u64*)sp, tst[0], __ATOMIC_RELAXED, __HIP_MEMORY_SCOPE_AGENT); __hip_atomic_store((GAS u64*)sp + 1, tst[AMPK] + (nsp & 0), __ATOMIC_RELAXED, __HIP_MEMORY_SCOPE_AGENT); } } while (0)
#define COUNT(sh) nsp += spins << (sh)

__global__ void __launch_bounds__(NT, 3) fused_k(Params p) {
    extern __shared__ __attribute__((aligned(16))) unsigned char lds_[];
    LAS unsigned char* L = (LAS unsigned char*)lds_;
    const unsigned LB = (unsigned)(uintptr_t)L;
    u64 pc0;
    asm volatile("s_getpc_b64 %0" : "=s"(pc0));
    const int b = blockIdx.x, tid = threadIdx.x, l = tid & 63, w = __builtin_amdgcn_readfirstlane(tid >> 6);
    const int g = l >> 4, i16 = l & 15;
    const int x = b & 7, i = b >> 3, isW = i & 1, pi = i >> 1, kc = x * 16 + pi, hh = isW;
    const rsrc_t rs = mkr(p.ws, W_END);
    const unsigned t0 = now32();
    const v4u ones = {SENT, SENT, SENT, SENT};
    u64 tst[NST]; unsigned nsp = 0;
#pragma unroll
    for (int i_ = 0; i_ < NST; ++i_) tst[i_] = 0;
    STAMP(0);
    if (w >= 8 && !isW) {
        const unsigned ln = (pi * 4 + w - 8) * 320;
        if (ln < CODE_KB * 1024) {
            const u64 ca = (pc0 & ~63ull) + ln;
            unsigned c0, c1, c2, c3, c4;
            asm volatile("s_load_dword %0, %5, 0x0\n\ts_load_dword %1, %5, 0x40\n\ts_load_dword %2, %5, 0x80\n\ts_load_dword %3, %5, 0xc0\n\ts_load_dword %4, %5, 0x100\n\ts_waitcnt lgkmcnt(0)"
                         : "=&s"(c0), "=&s"(c1), "=&s"(c2), "=&s"(c3), "=&s"(c4) : "s"(ca) : "memory");
        }
    }

    if (w >= 8) {
    const int cw = w - 8;
    __builtin_amdgcn_s_setprio(3);
    const v2u ep = ld64(rs, W_EP);
#define CLEAR() { _Pragma("unroll") for (int n = 0; n < NCLR; ++n) st128(rs, l * 16, W_OB + kc * 32768 + cw * 8192 + n * 1024, ones); \
        st128(rs, l < 32 ? l * 16 : OOB, W_LS + kc * 2048 + cw * 512, ones); \
        asm volatile("s_waitcnt vmcnt(0)" ::: "memory"); \
        st64(rs, l == 0 ? 0 : OOB, W_FL + kc * 128 + cw * 8, tag); }
    if (cw == 2) {
        s128(LB + L_MX + l * 16, ones);
        if (l < 36) s128(LB + L_MX + 1024 + l * 16, ones);
    }
    const u64 tag64 = mktag(ep);
    const v2u tag = {(unsigned)tag64, (unsigned)(tag64 >> 32)};
    if (!isW) { wait_until(t0, T_CLR); CLEAR() }
    STAMP(1);
    if (isW) { BAR(); BAR(); }
    {
        const int qb = W_QA + (16 * hh + 4 * cw) * 2048;
        int qv = l * 16;
        unsigned qw = LB + L_QA + 4 * cw * RS + l * 8;
        PIN(qv); PIN(qw);
        wait_until(t0, T_QA);
        STAMP(3);
        v4u q[8];
        unsigned bad;
#define QALOAD _Pragma("unroll") for (int n = 0; n < 8; ++n) q[n] = ld128(rs, qv, qb + n * 1024); \
        bad = 0; _Pragma("unroll") for (int n = 0; n < 8; ++n) { bad |= (q[n][1] ^ tag[1]) | (q[n][3] ^ tag[1]); s64(qw + (n >> 1) * RS + (n & 1) * 512, (v2u){q[n][0], q[n][2]}); }
        POLL(QALOAD, !__all(bad == 0), 4, 28);
    }
    STAMP(4);
    BAR();
    const int vh = 4 * x + (i >> 3), vs = i & 7;
    v4u uvr[4];
    {
        v4u f = ld128(rs, (l >> 1) * 128 + (l & 1) * 16, W_FL + 32 * cw * 128);
        const rsrc_t ru = mkr(p.wuv, 4 << 20);
#pragma unroll
        for (int n = 0; n < 4; ++n) uvr[n] = in128(ru, (l >> 2) * 8192 + (l & 3) * 16, (64 * vs + 16 * n) * 8192 + vh * 256 + cw * 64);
        SCHED();
#define FLBAD !__all((f[0] == tag[0]) & (f[1] == tag[1]) & (f[2] == tag[0]) & (f[3] == tag[1]))
        if (UNLIKELY(FLBAD)) {
            unsigned spins = 0;
            _Pragma("nounroll") do { __builtin_amdgcn_s_sleep(4); FENCE(); f = ld128(rs, (l >> 1) * 128 + (l & 1) * 16, W_FL + 32 * cw * 128); } while ((FLBAD) && ++spins < SPIN_CAP);
        }
        s32(LB + L_OK + cw * 4, 0u);
    }
    STAMP(2);
    __builtin_amdgcn_s_setprio(0);
    auto tail = [&](auto role_) __attribute__((always_inline)) {
    constexpr int ROLE = decltype(role_)::value;
    constexpr bool isV = ROLE == 1, isF = ROLE == 2;
    const int fc = 7 * x + pi - 8;
    v4u wf[16];
    unsigned xb = 0, b1v = 0; v2u b2v = {0, 0};
#define TRANSPOSE8(dst, src) _Pragma("unroll") for (int t = 0; t < 4; ++t) _Pragma("unroll") for (int c = 0; c < 4; ++c) { \
        (dst)[2 * c][t] = perm_lo((src)[2 * t][c], (src)[2 * t + 1][c]); (dst)[2 * c + 1][t] = perm_hi((src)[2 * t][c], (src)[2 * t + 1][c]); }
    if (isV) {
        const rsrc_t ro = mkr(p.wo, 4 << 20);
#pragma unroll
        for (int n = 0; n < 16; ++n) wf[n] = in128(ro, (i16 >> 3) * 32768 + g * 4096 + (i16 & 7) * 16 + ((n & 7) >> 1) * 1024, ((4 * x + cw) * 128 + 64 * (n >> 3) + 16 * (n & 1)) * 1024 + pi * 128);
    } else if (isF) {
        const rsrc_t r1 = mkr(p.w1, 512 * 1792 * 2), r2 = mkr(p.w2, 512 * 1792 * 2);
#pragma unroll
        for (int n = 0; n < 8; ++n) wf[n] = in128(r1, (l >> 2) * 3584 + (l & 3) * 16, (128 * cw + 16 * n) * 3584 + fc * 64);
#pragma unroll
        for (int j = 0; j < 8; ++j) wf[8 + j] = in128(r2, g * 8192 + i16 * 16 + (j & 3) * 1024, (32 * fc + (j & 4)) * 1024 + cw * 256);
        xb = *(const GAS unsigned*)(p.x + 128 * cw + 2 * l);
        b1v = p.b1[32 * fc + 8 * g + (i16 & 7)];
        b2v = *(const GAS v2u*)(p.b2 + 32 * (2 * x + (pi & 1)) + 8 * (l & 3) + 4 * ((l >> 2) & 1));
    }
    SCHED();
    {
        v4u selm;
#pragma unroll
        for (int t = 0; t < 4; ++t) selm[t] = (((l & 7) >> 1) == t) ? ((l & 1) ? 0xffff0000u : 0x0000ffffu) : 0u;
        int lsv = (4 * (l >> 3) + (l & 3)) * 128, obv = (l >> 3) * 512 + (l & 7) * 16;
        const int lss = W_LS + ((vh >> 4) * 128 + 32 * cw) * 128 + (vh & 15) * 8, obs = W_OB + (vh * 8 + vs) * 16384 + cw * 4096;
        unsigned wp = LB + L_SC + ((g >> 1) == (i16 >> 3) ? 2 * cw + (g >> 1) : 10 + cw) * 144 + (8 * (i16 & 7) + 4 * (g & 1)) * 2;
        unsigned rp = LB + L_SC + (i16 & 7) * 144 + 16 * g;
        unsigned sp = LB + L_SC + 1280 + cw * 4, sq = LB + L_SC + 1280;
        int cxv = l < 16 ? l * 4 : OOB;
        PIN(selm); PIN(lsv); PIN(obv); PIN(wp); PIN(rp); PIN(sp); PIN(sq); PIN(cxv);
        SCHED();
        STAMP(5);
        BAR();
        const unsigned tB = now32();
        v4u uvf[2][2];
        {
            const unsigned ub = LB + L_CT + cw * 8192;
#pragma unroll
            for (int n = 0; n < 4; ++n) s128(ub + (16 * n + (l >> 2)) * 80 + (l & 3) * 16, uvr[n]);
            const unsigned ur = ub + (16 * (i16 >> 2) + g) * 80 + (i16 & 3) * 8;
#pragma unroll
            for (int tl = 0; tl < 2; ++tl)
#pragma unroll
                for (int ks = 0; ks < 2; ++ks) {
                    const v2u a_ = ltr(ur + (4 * ks) * 80 + tl * 32), c_ = ltr(ur + (4 * ks + 8) * 80 + tl * 32);
                    uvf[tl][ks] = (v4u){a_[0], a_[1], c_[0], c_[1]};
                }
            PIN(uvf[0][0]); PIN(uvf[0][1]); PIN(uvf[1][0]); PIN(uvf[1][1]);
        }
        if (isV) {
#pragma unroll
            for (int hb = 0; hb < 2; ++hb) {
                v4u r[8];
#pragma unroll
                for (int n = 0; n < 8; ++n) r[n] = wf[8 * hb + n];
                TRANSPOSE8(wf + 8 * hb, r)
#pragma unroll
                for (int n = 0; n < 8; ++n) PIN(wf[8 * hb + n]);
                SCHED();
            }
        }
        SCHED();
        wait_until(tB, DLY_B);
        STAMP(10);
        f32x4 acc;
        unsigned mx;
#define V1TRY { const v2u ml = ld64(rs, lsv, lss); v4u od[4]; \
            _Pragma("unroll") for (int n = 0; n < 4; ++n) od[n] = ld128(rs, obv + n * 128, obs); \
            SCHED(); \
            const float m = __uint_as_float(ml[0]); \
            float M = fmx3(m, dpp<0xB1>(m), dpp<0x4E>(m)); \
            M = fmx(M, dpp<0xB1>(M)); \
            M = x32_max(x16_max(fmx(M, dpp<0x128>(M)))); \
            const float e = __builtin_amdgcn_exp2f(m - M); \
            const unsigned dup = pk(e, e); \
            const v4u a0 = sel4(dppu<0x00>(dup), selm), a1 = sel4(dppu<0x55>(dup), selm), a2 = sel4(dppu<0xAA>(dup), selm), a3 = sel4(dppu<0xFF>(dup), selm); \
            float Lw = bflo(dup) * __uint_as_float(ml[1]); \
            Lw += dpp<0xB1>(Lw); Lw += dpp<0x4E>(Lw); Lw += dpp<0x128>(Lw); \
            Lw = x32_sum(x16_sum(Lw)); \
            s32f(sp, M); s32f(sp + 16, Lw); \
            mx = max(ml[0], ml[1]); \
            SCHED(); \
            acc = mfma(a0, od[0], (f32x4){0.f, 0.f, 0.f, 0.f}); acc = mfma(a1, od[1], acc); acc = mfma(a2, od[2], acc); acc = mfma(a3, od[3], acc); \
            _Pragma("unroll") for (int n = 0; n < 4; ++n) mx = max(mx, umax4(od[n])); }
        POLL(V1TRY, !__all(mx != SENT), 1, 0);
        STAMP(6);
        s64(wp, (v2u){pk(acc[0], acc[1]), pk(acc[2], acc[3])});
        BAR();
        const v4u a0 = l128(rp), a1 = l128(rp + 64);
        const f32x4 Ms = asf(l128(sq)), Ls = asf(l128(sq + 16));
        f32x4 d0 = {0.f, 0.f, 0.f, 0.f}, d1 = {0.f, 0.f, 0.f, 0.f};
        d0 = mfma(a0, uvf[0][0], d0); d1 = mfma(a0, uvf[1][0], d1);
        d0 = mfma(a1, uvf[0][1], d0); d1 = mfma(a1, uvf[1][1], d1);
        const float Mg = max4(Ms);
        const f32x4 fw = {__builtin_amdgcn_exp2f(Ms[0] - Mg), __builtin_amdgcn_exp2f(Ms[1] - Mg), __builtin_amdgcn_exp2f(Ms[2] - Mg), __builtin_amdgcn_exp2f(Ms[3] - Mg)};
        const float Lr = __builtin_amdgcn_rcpf(sum4(fw * Ls));
        const float fa = ((g & 1) ? fw[2] : fw[0]) * Lr, fb = ((g & 1) ? fw[3] : fw[1]) * Lr;
        const float r0 = x16_sum(fa * (d0[0] + d0[1]) + fb * (d0[2] + d0[3])), r1 = x16_sum(fa * (d1[0] + d1[1]) + fb * (d1[2] + d1[3]));
        st32(rs, cxv, W_CX + (vh * 8 + vs) * 256 + cw * 64, san(pk(r0, r1)));
    }
    const unsigned tV = now32();
    STAMP(7);
    if (isV) {
        const int cbs = W_CX + (4 * x + cw) * 2048;
        int cbv = l * 16, opv = l < 16 ? l * 16 : OOB;
        unsigned wp = LB + L_CT + cw * 4096 + (l >> 4) * 272 + i16 * 16;
        unsigned rp = LB + L_CT + cw * 4096 + (i16 & 7) * 272 + (i16 >> 3) * 64 + g * 16;
        unsigned ow = LB + L_QA + ((g >> 1) == (i16 >> 3) ? 2 * cw + (g >> 1) : 8 + cw) * 272 + (i16 & 7) * 32 + (g & 1) * 16, orr = LB + L_QA + g * 544 + i16 * 16;
        PIN(cbv); PIN(opv); PIN(wp); PIN(rp); PIN(ow); PIN(orr);
#pragma unroll
        for (int t = 0; t < 16; ++t) PIN(wf[t]);
        SCHED();
        wait_until(tV, DLY_V);
        v4u c0, c1;
        POLL(c0 = ld128(rs, cbv, cbs); c1 = ld128(rs, cbv + 1024, cbs), !__all(max(umax4(c0), umax4(c1)) != SENT), 1, 8);
        STAMP(8);
        s128(wp, c0); s128(wp + 4 * 272, c1);
        const v4u cx0 = l128(rp), cx1 = l128(rp + 128);
        f32x4 d[8];
#pragma unroll
        for (int t = 0; t < 8; ++t) d[t] = mfma(cx0, wf[t], (f32x4){0.f, 0.f, 0.f, 0.f});
#pragma unroll
        for (int t = 0; t < 8; ++t) d[t] = mfma(cx1, wf[8 + t], d[t]);
        v4u e;
#pragma unroll
        for (int t = 0; t < 4; ++t) {
            auto s = __builtin_amdgcn_permlane16_swap(__float_as_uint(sum4(d[t])), __float_as_uint(sum4(d[t + 4])), false, false);
            e[t] = __float_as_uint(__uint_as_float(s[0]) + __uint_as_float(s[1]));
        }
        s128(ow, e);
        BAR();
        if (cw == 0) {
            const f32x4 s = asf(l128(orr)) + asf(l128(orr + 272));
            float s0 = s[0], s1 = s[1], s2 = s[2], s3 = s[3];
            red2_rows(s0, s1); red2_rows(s2, s3);
            st128(rs, opv, W_OP + x * 2048 + pi * 256, (v4u){san(__float_as_uint(s0)), san(__float_as_uint(s1)), san(__float_as_uint(s2)), san(__float_as_uint(s3))});
        }
        STAMP(13);
        STAMP_OUT();
        return;
    }
    if (!isF) { STAMP_OUT(); return; }
    {
        const unsigned ub = LB + L_CT + 32768 + cw * 10240;
#pragma unroll
        for (int n = 0; n < 8; ++n) s128(ub + (16 * n + (l >> 2)) * 80 + (l & 3) * 16, wf[n]);
        const unsigned ur = ub + (8 * g + (i16 >> 2)) * 80 + (i16 & 3) * 8;
#pragma unroll
        for (int tl = 0; tl < 2; ++tl)
#pragma unroll
            for (int ks = 0; ks < 4; ++ks) {
                const v2u a_ = ltr(ur + (32 * ks) * 80 + tl * 32), c_ = ltr(ur + (32 * ks + 4) * 80 + tl * 32);
                wf[4 * tl + ks] = (v4u){a_[0], a_[1], c_[0], c_[1]};
            }
        v4u q[8];
#pragma unroll
        for (int n = 0; n < 8; ++n) q[n] = wf[8 + n];
        TRANSPOSE8(wf + 8, q)
    }
    {
        const v4u one = {0x3f803f80u, 0x3f803f80u, 0x3f803f80u, 0x3f803f80u};
        f32x4 d0 = {0.f, 0.f, 0.f, 0.f}, d1 = {0.f, 0.f, 0.f, 0.f};
#pragma unroll
        for (int ks = 0; ks < 4; ++ks) { d0 = mfma(one, wf[ks], d0); d1 = mfma(one, wf[4 + ks], d1); }
        s32f(LB + L_SC + 2560 + i16 * 16 + cw * 4, d0[0]); s32f(LB + L_SC + 2560 + (16 + i16) * 16 + cw * 4, d1[0]);
    }
    {
        int opv = l * 8;
        unsigned hw = LB + L_P + cw * 544 + l * 4, hr = LB + L_P + cw * 544 + (i16 & 1) * 272 + g * 16;
        unsigned hf = LB + L_QA + (128 * cw + 2 * l) * 4;
        unsigned uw = LB + L_SC + 3072 + i16 * 16 + cw * 4, ur = LB + L_SC + 2560 + (8 * g + (i16 & 7)) * 16;
        unsigned sw = LB + L_SC + 3584 + cw * 4, sr = LB + L_SC + 3584;
        int ypv = (g & 1) ? OOB : (i16 >> 2) * (56 * 128) + (i16 & 3) * 16 + (g >> 1) * 64;
        float x0 = bflo(xb), x1 = bfhi(xb), b1f = bflo(b1v);
        PIN(opv); PIN(hw); PIN(hr); PIN(hf); PIN(uw); PIN(ur); PIN(sw); PIN(sr); PIN(ypv); PIN(x0); PIN(x1); PIN(b1f);
#pragma unroll
        for (int t = 0; t < 16; ++t) PIN(wf[t]);
        SCHED();
        wait_until(tV, DLY_C);
        v2u op[8];
        unsigned mx;
#define OPLOAD _Pragma("unroll") for (int n = 0; n < 8; ++n) op[n] = ld64(rs, opv, W_OP + n * 2048 + cw * 512); \
        mx = 0; _Pragma("unroll") for (int n = 0; n < 8; ++n) mx = max(mx, max(op[n][0], op[n][1]));
        POLL(OPLOAD, !__all(mx != SENT), 1, 8);
        STAMP(8);
        f32x2 h = {x0, x1};
#pragma unroll
        for (int n = 0; n < 8; ++n) h += __builtin_bit_cast(f32x2, op[n]);
        const float mw = wave_sum(h[0] + h[1]) * (1.f / 128.f);
        const float c0 = h[0] - mw, c1 = h[1] - mw;
        const unsigned hi = pk(c0, c1), lo = pk(c0 - bflo(hi), c1 - bfhi(hi));
        s32(hw, hi); s32(hw + 272, lo);
        v4u a[4];
#pragma unroll
        for (int ks = 0; ks < 4; ++ks) a[ks] = l128v(hr + ks * 64);
        s64(hf, __builtin_bit_cast(v2u, h));
        SCHED();
        const float m2 = wave_sum(c0 * c0 + c1 * c1);
        f32x4 d0 = {0.f, 0.f, 0.f, 0.f}, d1 = {0.f, 0.f, 0.f, 0.f};
#pragma unroll
        for (int ks = 0; ks < 4; ++ks) { d0 = mfma(a[ks], wf[ks], d0); d1 = mfma(a[ks], wf[4 + ks], d1); }
        s32f(sw, mw); s32f(sw + 16, m2);
        s32f(uw, d0[0] + d0[1]); s32f(uw + 256, d1[0] + d1[1]);
        BAR();
        const f32x4 mm = asf(l128(sr)), qq = asf(l128(sr + 16));
        const f32x4 cc = asf(l128(ur)), uu = asf(l128(ur + 512));
        const float mean = sum4(mm) * 0.25f;
        const f32x4 dm = mm - mean;
        const float var = (sum4(qq) + 128.f * sum4(dm * dm)) * (1.f / 512.f);
        const float rstd = __builtin_amdgcn_rsqf(var + 1e-5f);
        const float u = (sum4(uu) + sum4(dm * cc)) * rstd + b1f;
        const float z = u * (1.f + 0.044715f * u * u) * (-2.f * 0.7978845608028654f * 1.4426950408889634f);
        const float ge = u * __builtin_amdgcn_rcpf(1.f + __builtin_amdgcn_exp2f(z));
        const unsigned pr = pk(ge, dpp<0x101>(ge));
        const v4u fa = {pr, dppu<0x102>(pr), dppu<0x104>(pr), dppu<0x106>(pr)};
        float y[8];
#pragma unroll
        for (int t = 0; t < 8; ++t) { f32x4 d = {0.f, 0.f, 0.f, 0.f}; d = mfma(fa, wf[8 + t], d); y[t] = d[0]; }
        v4u v;
#pragma unroll
        for (int t = 0; t < 4; ++t) v[t] = san(__float_as_uint(g ? y[4 + t] : y[t]));
        st128(rs, ypv, W_YP + (4 * cw * 56 + fc) * 128, v);
    }
    const unsigned tF = now32();
    STAMP(13);
    if (pi < 10 && cw == 0) {
        const int fin = 2 * x + (pi & 1);
        const int col = 32 * fin + 8 * (l & 3) + 4 * ((l >> 2) & 1);
        f32x4 hv = asf(l128(LB + L_QA + col * 4));
        hv += (f32x4){bflo(b2v[0]), bfhi(b2v[0]), bflo(b2v[1]), bfhi(b2v[1])};
        const rsrc_t rout = mkr(p.out, 1024);
        int yv_ = l * 16, ov = l < 8 ? col * 2 : OOB;
        PIN(hv); PIN(yv_); PIN(ov);
        SCHED();
        wait_until(tF, DLY_D);
        v4u yv[7];
        unsigned mx;
#define YLOAD _Pragma("unroll") for (int n = 0; n < 7; ++n) yv[n] = ld128(rs, yv_, W_YP + fin * 56 * 128 + n * 1024); \
        mx = 0; _Pragma("unroll") for (int n = 0; n < 7; ++n) mx = max(mx, umax4(yv[n]));
        POLL(YLOAD, !__all(mx != SENT), 1, 16);
        STAMP(12);
        f32x4 s = asf(yv[0]);
#pragma unroll
        for (int n = 1; n < 7; ++n) s += asf(yv[n]);
        float s0 = s[0], s1 = s[1], s2 = s[2], s3 = s[3];
        red2_rows(s0, s1); red2_rows(s2, s3);
        s0 += dpp<0x128>(s0); s1 += dpp<0x128>(s1); s2 += dpp<0x128>(s2); s3 += dpp<0x128>(s3);
        __builtin_amdgcn_raw_buffer_store_b64((v2u){pk(hv[0] + s0, hv[1] + s1), pk(hv[2] + s2, hv[3] + s3)}, rout, ov, 0, 0);
        if (fin == 0) st64(rs, l == 0 ? 0 : OOB, W_EP, tag);

        if (fin == 0) {
            const u64 tend = __builtin_amdgcn_s_memrealtime();
            STAMP_OUT();
            while (__builtin_amdgcn_s_memrealtime() < tend + 500) __builtin_amdgcn_s_sleep(2);
            u64 mn = ~0ull, mxv = 0;
            for (int e = l; e < 256 * 12; e += 64) {
                const u64 a = __hip_atomic_load((GAS u64*)(p.ws + W_ST) + e * 2, __ATOMIC_RELAXED, __HIP_MEMORY_SCOPE_AGENT);
                const u64 c = __hip_atomic_load((GAS u64*)(p.ws + W_ST) + e * 2 + 1, __ATOMIC_RELAXED, __HIP_MEMORY_SCOPE_AGENT);
                mn = a < mn ? a : mn;
                const int ww = e % 12;
                if ((AMPC == 0 && ww < 8) || (AMPC == 1 && ww >= 8)) mxv = c > mxv ? c : mxv;
            }
            for (int o = 32; o > 0; o >>= 1) { u64 t = __shfl_xor(mn, o); mn = t < mn ? t : mn; t = __shfl_xor(mxv, o); mxv = t > mxv ? t : mxv; }
            if (AMPC == 3) mxv = tend;
            u64 dur = AMPC == 2 ? 0 : mxv - mn;
            if (dur > 5000) dur = 5000;
            const u64 until = tend + 2000 + 10 * dur;
            while (__builtin_amdgcn_s_memrealtime() < until) __builtin_amdgcn_s_sleep(2);
        }
    }
    STAMP_OUT();
    };
    if (!isW && pi < 8) { tail(IC<1>{}); __builtin_amdgcn_endpgm(); }
    if (!isW && pi < 15) { tail(IC<2>{}); __builtin_amdgcn_endpgm(); }
    tail(IC<0>{});
    __builtin_amdgcn_endpgm();
    }
    {
        const int hq = x * 4 + (i >> 3), cq = (i & 7) >> 1;
        unsigned tag32 = 0;
#define UKDMA() { const rsrc_t rk = mkr(p.wuk, 4 << 20); _Pragma("unroll") for (int n = 0; n < 4; ++n) \
            __builtin_amdgcn_raw_ptr_buffer_load_lds(rk, (LAS void*)(L + L_CT + 16 * w * RS + n * 1024), 16, g * 8192 + ((i16 ^ (4 * n + g)) * 16), \
                                                     hq * 256 + (128 * cq + 16 * w + 4 * n) * 8192, 0, 0); }
        if (__builtin_expect(w < 2, 1)) {
            const rsrc_t rq = mkr(p.wq, 4 << 20), rx = mkr(p.x, 1024);
            const v4u xs = in128(rx, l * 16, 0);
            const v4u xr = in128(rx, g * 16, (i & 7) * 128 + w * 64);
            const v2u ep = ld64(rs, W_EP);
            SCHED();
            v4u wq[8];
#pragma unroll
            for (int n = 0; n < 8; ++n) wq[n] = in128(rq, g * 65536 + i16 * 16, hq * 256 + (64 * (i & 7) + 32 * w + n) * 8192);
            SCHED();
            v4u selm;
#pragma unroll
            for (int t = 0; t < 4; ++t) selm[t] = (((l & 7) >> 1) == t) ? ((l & 1) ? 0xffff0000u : 0x0000ffffu) : 0u;
            int qpv = l < 32 ? l * 16 : OOB;
            float xv[8];
#pragma unroll
            for (int t = 0; t < 4; ++t) { xv[2 * t] = bflo(xs[t]); xv[2 * t + 1] = bfhi(xs[t]); }
            float sm = 0.f;
#pragma unroll
            for (int t = 0; t < 8; ++t) sm += xv[t];
            const float mean = wave_sum(sm) * (1.f / 512.f);
            float sq = 0.f;
#pragma unroll
            for (int t = 0; t < 8; ++t) { const float d = xv[t] - mean; sq += d * d; }
            const float rstd = __builtin_amdgcn_rsqf(wave_sum(sq) * (1.f / 512.f) + 1e-5f);
            v4u xa[8];
#pragma unroll
            for (int n = 0; n < 8; ++n) {
                const float xn = (((n & 1) ? bfhi(xr[n >> 1]) : bflo(xr[n >> 1])) - mean) * rstd;
                xa[n] = sel4(pk(xn, xn), selm);
            }
            tag32 = (unsigned)(mktag(ep) >> 32);
            PIN(qpv);
            SCHED();
            f32x4 acc = {0.f, 0.f, 0.f, 0.f};
#pragma unroll
            for (int n = 0; n < 8; ++n) acc = mfma(xa[n], wq[n], acc);
            st128(rs, qpv, W_QP + (hq * 16 + (i & 7) * 2 + w) * 512, (v4u){pk(acc[0], acc[1]), tag32, pk(acc[2], acc[3]), tag32});
            STAMP(1);
        }
        if (isW) {
            UKDMA()
            if (w >= 2) { const v2u ep = ld64(rs, W_EP); tag32 = (unsigned)(mktag(ep) >> 32); }
            const int kr = 4 * (i16 & 3) + (i16 >> 2);
            unsigned wp = LB + L_SC + (2 * w + (l >> 5)) * 272 + i16 * 16 + (g & 1) * 8;
            unsigned rp = LB + L_SC + i16 * 272 + 16 * g;
            unsigned kp = LB + L_CT + 16 * w * RS + kr * 256;
            int qv = l * 16, qav = l < 4 ? l * 16 : OOB;
            PIN(wp); PIN(rp); PIN(qv); PIN(qav); PIN(kp);
            SCHED();
            wait_until(t0, T_QP);
            v4u qg;
            POLL(qg = ld128(rs, qv, W_QP + (hq * 16 + 2 * w) * 512), !__all((qg[1] == tag32) & (qg[3] == tag32)), 1, 24);
            s64(wp, (v2u){qg[0], qg[2]});
            v4u uk[4];
#pragma unroll
            for (int s = 0; s < 4; ++s) uk[s] = l128v(kp + (((4 * s + g) ^ kr) * 16));
            STAMP(9);
            BAR();
            v4u a[4];
#pragma unroll
            for (int s = 0; s < 4; ++s) a[s] = l128(rp + 64 * s);
            f32x4 d0 = {0.f, 0.f, 0.f, 0.f};
#pragma unroll
            for (int s = 0; s < 4; ++s) d0 = mfma(a[s], uk[s], d0);
            constexpr float QS = 0.08838834764831845f * 1.4426950408889634f;
            const float r0 = x32_sum(x16_sum(sum4(d0))) * QS;
            const unsigned pr = pk(r0, dpp<0x104>(r0)), pr2 = dppu<0x108>(pr);
            st128(rs, qav, W_QA + hq * 2048 + cq * 512 + w * 64, (v4u){pr, tag32, pr2, tag32});
            STAMP(2);
            BAR();
        } else {
            wait_until(t0, T_C);
        }
        const int wk = w;
        {
            const rsrc_t rc = mkr(p.ckv, 16 << 20);
#pragma unroll
            for (int r = 0; r < 16; ++r)
                __builtin_amdgcn_raw_ptr_buffer_load_lds(rc, (LAS void*)(L + L_CT + (16 * w + r) * RS), 16, l * 16, (kc * 128 + 16 * w + r) * 1024, 0, 0);
        }
        const int rho = 8 * (i16 >> 3) + 2 * (i16 & 3) + ((i16 >> 2) & 1);
        unsigned qp = LB + L_QA + i16 * RS + g * 256;
        unsigned cp = LB + L_CT + (16 * wk + rho) * RS + g * 256;
        unsigned tp0 = LB + L_CT + (8 * g + 2 * (i16 >> 2)) * RS + 128 * w + 8 * (i16 & 3);
        unsigned tp1 = tp0 + 64 * RS;
        unsigned mo = LB + L_MX + i16 * 48 + (g >> 1) * 16, mx_ = LB + L_MX + i16 * 48 + (1 - (g >> 1)) * 16;
        unsigned mq = LB + L_MX + i16 * 48 + (wk & 1) * 16 + (wk >> 1) * 4;
        unsigned pw = LB + L_P + i16 * PS + (16 * wk + 4 * g) * 2;
        unsigned pr = LB + L_P + i16 * PS + g * 16;
        int obv = (4 * g + 2 * (i16 >> 3)) * (8 * 16384) + (i16 & 7) * 16;
        unsigned okp = LB + L_OK;
        PIN(qp); PIN(cp); PIN(tp0); PIN(tp1); PIN(mo); PIN(mx_); PIN(mq); PIN(pw); PIN(pr); PIN(obv); PIN(okp);
        STAMP(3);
        BAR();
        STAMP(4);
        v4u qf[16], cr[16];
#pragma unroll
        for (int s = 0; s < 16; ++s) qf[s] = l128(qp + s * 16);
        SCHED();
        __builtin_amdgcn_s_waitcnt(0x0F70);
        FENCE();
        STAMP(5);
#pragma unroll
        for (int s = 0; s < 16; ++s) cr[s] = l128(cp + s * 16);
        SCHED();
        f32x4 sc = {0.f, 0.f, 0.f, 0.f};
#pragma unroll
        for (int s = 0; s < 16; ++s) sc = mfma(cr[s], qf[s], sc);
        const float mw = __builtin_ceilf(x32_max(x16_max(max4(sc))));
        {
            const unsigned p0 = pk(__builtin_amdgcn_exp2f(sc[0] - mw), __builtin_amdgcn_exp2f(sc[1] - mw)), p1 = pk(__builtin_amdgcn_exp2f(sc[2] - mw), __builtin_amdgcn_exp2f(sc[3] - mw));
            s64(pw, (v2u){p0, p1});
            const bf16x2 one2 = __builtin_bit_cast(bf16x2, 0x3f803f80u);
            float ls = __builtin_amdgcn_fdot2_f32_bf16(__builtin_bit_cast(bf16x2, p0), one2, 0.f, false);
            ls = __builtin_amdgcn_fdot2_f32_bf16(__builtin_bit_cast(bf16x2, p1), one2, ls, false);
            s32(mq, san(__float_as_uint(mw)));
            ls = x32_sum(x16_sum(ls));
            s32(mq + 768, san(__float_as_uint(ls)));
        }
        STAMP(6);
        v4u m0, m1, pf[4];
#define MXLOAD m0 = l128v(mo); m1 = l128v(mx_); _Pragma("unroll") for (int ks = 0; ks < 4; ++ks) pf[ks] = l128v(pr + ks * 64)
        POLL(MXLOAD, !__all(max(umax4(m0), umax4(m1)) != SENT), 1, 0);
        FENCE();
        STAMP(7);
        v4u cf[4][4];
#define TRREAD(ks) _Pragma("unroll") for (int ct = 0; ct < 4; ++ct) { \
            const unsigned t_ = ((ks) < 2 ? tp0 : tp1) + (32 * ((ks) & 1)) * RS + 32 * ct; \
            const v2u a_ = ltr(t_), c_ = ltr(t_ + RS); \
            cf[ks][ct] = (v4u){a_[0], a_[1], c_[0], c_[1]}; }
        TRREAD(0) TRREAD(1)
        v4u okw = l128v(okp);
        SCHED();
        const float M = fmx(max4(asf(m0)), max4(asf(m1)));
#pragma unroll
        for (int ks = 0; ks < 4; ++ks) {
            const unsigned short dl = (unsigned short)(unsigned)__builtin_fminf((M - __uint_as_float(m0[ks])) * 128.f, 32640.f);
#pragma unroll
            for (int t = 0; t < 4; ++t) pf[ks][t] = pksubsat(pf[ks][t], dl);
        }
        SCHED();
        TRREAD(2) TRREAD(3)
        SCHED();
        f32x4 o[4];
#pragma unroll
        for (int ct = 0; ct < 4; ++ct) o[ct] = (f32x4){0.f, 0.f, 0.f, 0.f};
#pragma unroll
        for (int ks = 0; ks < 2; ++ks)
#pragma unroll
            for (int ct = 0; ct < 4; ++ct) o[ct] = mfma(pf[ks], cf[ks][ct], o[ct]);
        SCHED();
        if (UNLIKELY(__builtin_amdgcn_readfirstlane(umax4(okw)) == SENT)) {
            unsigned spins = 0;
            _Pragma("nounroll") do { __builtin_amdgcn_s_sleep(1); okw = l128v(okp); } while (__builtin_amdgcn_readfirstlane(umax4(okw)) == SENT && ++spins < SPIN_CAP);
        }
        FENCE();
        SCHED();
#pragma unroll
        for (int ks = 2; ks < 4; ++ks)
#pragma unroll
            for (int ct = 0; ct < 4; ++ct) o[ct] = mfma(pf[ks], cf[ks][ct], o[ct]);
        STAMP(11);
        const int obs = W_OB + (16 * hh * 8 + w) * 16384 + kc * 128;
#pragma unroll
        for (int r = 0; r < 2; ++r) {
            v4u v;
#pragma unroll
            for (int t = 0; t < 2; ++t) {
                const unsigned A = san(pk(o[2 * t][r], o[2 * t + 1][r])), B = san(pk(o[2 * t][r + 2], o[2 * t + 1][r + 2]));
                v[t] = (unsigned)__builtin_amdgcn_update_dpp((int)A, (int)B, 0x128, 0xf, 0xC, false);
                v[2 + t] = (unsigned)__builtin_amdgcn_update_dpp((int)B, (int)A, 0x128, 0xf, 0x3, false);
            }
            st128(rs, obv, obs + r * (8 * 16384), v);
        }
        if (w == 0) {
            v4u l0, l1;
            POLL(l0 = l128v(mo + 768); l1 = l128v(mx_ + 768), !__all(max(umax4(l0), umax4(l1)) != SENT), 1, 0);
            float lt = 0.f;
#pragma unroll
            for (int t = 0; t < 4; ++t) lt += __uint_as_float(l0[t]) * __builtin_amdgcn_exp2f(__uint_as_float(m0[t]) - M) + __uint_as_float(l1[t]) * __builtin_amdgcn_exp2f(__uint_as_float(m1[t]) - M);
            st64(rs, l < 16 ? l * 8 : OOB, W_LS + (hh * 128 + kc) * 128, (v2u){san(__float_as_uint(M)), san(__float_as_uint(lt))});
        }
        STAMP(8);
        STAMP_OUT();
    }
}

extern "C" void kernel_launch(void* const* d_in, const int* in_sizes, int n_in, void* d_out, int out_size, void* d_ws, size_t ws_size,
                              hipStream_t stream) {
    Params p;
    p.x = (const u16*)d_in[0]; p.ckv = (const u16*)d_in[1]; p.wq = (const u16*)d_in[2]; p.wuk = (const u16*)d_in[3];
    p.wuv = (const u16*)d_in[4]; p.wo = (const u16*)d_in[5]; p.w1 = (const u16*)d_in[7]; p.b1 = (const u16*)d_in[8];
    p.w2 = (const u16*)d_in[9]; p.b2 = (const u16*)d_in[10]; p.out = (u16*)d_out; p.ws = (unsigned char*)d_ws + WS_OFF(ws_size);
    (void)hipFuncSetAttribute((const void*)fused_k, hipFuncAttributeMaxDynamicSharedMemorySize, L_END);
    hipLaunchKernelGGL(fused_k, dim3(NWG), dim3(NT), L_END, stream, p);
}
```

```cpp
#include <hip/hip_runtime.h>
#include <stdint.h>

typedef unsigned short u16;
typedef unsigned long long u64;
typedef unsigned v4u __attribute__((ext_vector_type(4)));
typedef unsigned v2u __attribute__((ext_vector_type(2)));
typedef float f32x4 __attribute__((ext_vector_type(4)));
typedef float f32x2 __attribute__((ext_vector_type(2)));
typedef __bf16 bf16x8 __attribute__((ext_vector_type(8)));
typedef __bf16 bf16x2 __attribute__((ext_vector_type(2)));
typedef short s16x4 __attribute__((ext_vector_type(4)));
#define GAS __attribute__((address_space(1)))
#define LAS __attribute__((address_space(3)))
#define DI __device__ __forceinline__

#define STAMPS 0
#define KNOB(i, v) (v)
#define T_C KNOB(0, 130)
#define T_CLR KNOB(1, 130)
#define T_QP KNOB(2, 170)
#define T_QA KNOB(3, 400)
#define DLY_B KNOB(4, 0)
#define DLY_V KNOB(5, 15)
#define DLY_C KNOB(6, 100)
#define DLY_D KNOB(7, 15)
#define NCLR 8
#define WS_TAIL 1
#define WS_OFF(sz) (WS_TAIL ? (((sz) - (16u << 20)) & ~(size_t)((2u << 20) - 1)) : 0)
#define CODE_KB 19

constexpr int NWG = 256, NT = 768;
constexpr int W_EP = 0, W_FL = 4096, W_QA = 65536, W_LS = W_QA + 65536, W_CX = W_LS + 32768, W_OP = W_CX + 65536,
              W_YP = W_OP + 16384, W_SMALL_END = W_LS + 128 * 2048, W_QP = W_SMALL_END, W_OB = 1 << 20, W_END = W_OB + (4 << 20), W_ST = 8 << 20;
static_assert(W_YP + 16 * 56 * 128 <= W_SMALL_END && W_QP + 262144 <= W_OB, "ws map");
constexpr int RS = 1040, L_CT = 0, L_QA = 128 * RS, L_P = L_QA + 16 * RS, PS = 272, L_MX = L_P + 16 * PS, L_LS = L_MX + 768,
              L_OK = L_LS + 768, L_CN = L_OK + 16, L_SC = L_OK + 64, L_END = L_SC + 4352;
constexpr unsigned SENT = 0xFFFFFFFFu;
constexpr unsigned SPIN_CAP = 200000;

struct Params {
    const u16 *x, *ckv, *wq, *wuk, *wuv, *wo, *w1, *b1, *w2, *b2;
    u16* out;
    unsigned char* ws;
};

DI float bflo(unsigned u) { return __uint_as_float(u << 16); }
DI float bfhi(unsigned u) { return __uint_as_float(u & 0xffff0000u); }
DI unsigned pk(float a, float b) { f32x2 f = {a, b}; return __builtin_bit_cast(unsigned, __builtin_convertvector(f, bf16x2)); }
DI unsigned san(unsigned u) { return u < 0xFFFFFFFEu ? u : 0xFFFFFFFEu; }
DI f32x4 mfma(v4u a, v4u b, f32x4 c) { return __builtin_amdgcn_mfma_f32_16x16x32_bf16(__builtin_bit_cast(bf16x8, a), __builtin_bit_cast(bf16x8, b), c, 0, 0, 0); }
template <int CTRL> DI float dpp(float v) { return __int_as_float(__builtin_amdgcn_update_dpp(0, __float_as_int(v), CTRL, 0xf, 0xf, true)); }
template <int CTRL> DI unsigned dppu(unsigned v) { return (unsigned)__builtin_amdgcn_update_dpp(0, (int)v, CTRL, 0xf, 0xf, true); }
DI float fmx(float a, float b) { return __builtin_elementwise_maximum(a, b); }
DI float fmx3(float a, float b, float c) { return fmx(fmx(a, b), c); }
DI float x16_sum(float v) { auto r = __builtin_amdgcn_permlane16_swap(__float_as_uint(v), __float_as_uint(v), false, false); return __uint_as_float(r[0]) + __uint_as_float(r[1]); }
DI float x32_sum(float v) { auto r = __builtin_amdgcn_permlane32_swap(__float_as_uint(v), __float_as_uint(v), false, false); return __uint_as_float(r[0]) + __uint_as_float(r[1]); }
DI float x16_max(float v) { auto r = __builtin_amdgcn_permlane16_swap(__float_as_uint(v), __float_as_uint(v), false, false); return fmx(__uint_as_float(r[0]), __uint_as_float(r[1])); }
DI float x32_max(float v) { auto r = __builtin_amdgcn_permlane32_swap(__float_as_uint(v), __float_as_uint(v), false, false); return fmx(__uint_as_float(r[0]), __uint_as_float(r[1])); }
DI float wave_sum(float v) {
    v += dpp<0xB1>(v); v += dpp<0x4E>(v); v += dpp<0x141>(v); v += dpp<0x140>(v);
    return x32_sum(x16_sum(v));
}
DI void red2_rows(float& a, float& b) {
    auto r = __builtin_amdgcn_permlane32_swap(__float_as_uint(a), __float_as_uint(b), false, false);
    float s = __uint_as_float(r[0]) + __uint_as_float(r[1]);
    s = x16_sum(s);
    auto q = __builtin_amdgcn_permlane32_swap(__float_as_uint(s), __float_as_uint(s), false, false);
    a = __uint_as_float(q[0]); b = __uint_as_float(q[1]);
}
DI float sum4(f32x4 v) { return (v[0] + v[1]) + (v[2] + v[3]); }
DI float max4(f32x4 v) { return fmx(fmx3(v[0], v[1], v[2]), v[3]); }
DI unsigned umax4(v4u v) { return max(max(v[0], v[1]), max(v[2], v[3])); }
DI f32x4 asf(v4u v) { return __builtin_bit_cast(f32x4, v); }
#define BAR() do { asm volatile("s_waitcnt lgkmcnt(0)" ::: "memory"); __builtin_amdgcn_s_barrier(); asm volatile("" ::: "memory"); } while (0)
#define FENCE() asm volatile("" ::: "memory")
#define SCHED() __builtin_amdgcn_sched_barrier(0)
#define PIN(v) asm volatile("" : "+v"(v))
typedef __amdgpu_buffer_rsrc_t rsrc_t;
DI rsrc_t mkr(const void* p, int bytes) { return __builtin_amdgcn_make_buffer_rsrc((void*)p, 0, bytes, 0x00020000); }
DI v4u ld128(rsrc_t r, int off, int so = 0) { return __builtin_amdgcn_raw_buffer_load_b128(r, off, so, 16); }
DI v2u ld64(rsrc_t r, int off, int so = 0) { return __builtin_amdgcn_raw_buffer_load_b64(r, off, so, 16); }
DI unsigned ld32(rsrc_t r, int off, int so = 0) { return __builtin_amdgcn_raw_buffer_load_b32(r, off, so, 16); }
DI void st128(rsrc_t r, int off, int so, v4u v) {
    __builtin_amdgcn_raw_buffer_store_b128(v, r, off, so, 16);
    asm volatile("s_nop 1" :: "v"(v) : "memory");
}
DI void st64(rsrc_t r, int off, int so, v2u v) { __builtin_amdgcn_raw_buffer_store_b64(v, r, off, so, 16); }
DI void st32(rsrc_t r, int off, int so, unsigned v) { __builtin_amdgcn_raw_buffer_store_b32(v, r, off, so, 16); }
DI v4u in128(rsrc_t r, int off, int so) { return __builtin_amdgcn_raw_buffer_load_b128(r, off, so, 0); }
DI v2u in64(rsrc_t r, int off, int so) { return __builtin_amdgcn_raw_buffer_load_b64(r, off, so, 0); }
DI unsigned in32(rsrc_t r, int off, int so) { return __builtin_amdgcn_raw_buffer_load_b32(r, off, so, 0); }
DI unsigned perm_lo(unsigned a, unsigned b) { return __builtin_amdgcn_perm(b, a, 0x05040100u); }
DI unsigned perm_hi(unsigned a, unsigned b) { return __builtin_amdgcn_perm(b, a, 0x07060302u); }
DI u64 mktag(v2u ep) {
    const u64 e = ((u64)(unsigned)__builtin_amdgcn_readfirstlane(ep[1]) << 32) | (unsigned)__builtin_amdgcn_readfirstlane(ep[0]);
    return e + 0x9E3779B97F4A7C15ull;
}
DI unsigned now32() { return (unsigned)__builtin_amdgcn_s_memrealtime(); }
DI void wait_until(unsigned t0, unsigned ticks) { while (now32() - t0 < ticks) __builtin_amdgcn_s_sleep(1); }
DI v4u l128(unsigned a) { return *(const LAS v4u*)a; }
DI v4u l128v(unsigned a) { return *(const volatile LAS v4u*)a; }
DI void s128(unsigned a, v4u v) { *(LAS v4u*)a = v; }
DI void s64(unsigned a, v2u v) { *(volatile LAS v2u*)a = v; }
DI void s32(unsigned a, unsigned v) { *(volatile LAS unsigned*)a = v; }
DI void s32f(unsigned a, float v) { *(volatile LAS float*)a = v; }
DI v2u ltr(unsigned a) { return __builtin_bit_cast(v2u, __builtin_amdgcn_ds_read_tr16_b64_v4i16((LAS s16x4*)a)); }
typedef unsigned short u16x2 __attribute__((ext_vector_type(2)));
DI unsigned pksubsat(unsigned a, unsigned short d) { return __builtin_bit_cast(unsigned, __builtin_elementwise_sub_sat(__builtin_bit_cast(u16x2, a), (u16x2){d, d})); }
template <int N> struct IC { static constexpr int value = N; };
#define UNLIKELY(c) __builtin_expect(!!(c), 0)
#define POLL(LOADS, BAD, SLEEP, SH) do { LOADS; if (UNLIKELY(BAD)) { unsigned spins = 0; _Pragma("nounroll") do { __builtin_amdgcn_s_sleep(SLEEP); FENCE(); LOADS; ++spins; } while ((BAD) && spins < SPIN_CAP); COUNT(SH); } } while (0)
constexpr int OOB = 0x7ffffff0;
DI v4u sel4(unsigned d, v4u m) { return (v4u){d & m[0], d & m[1], d & m[2], d & m[3]}; }

#define STAMP(i)
#define STAMP_OUT()
#define COUNT(sh)

__global__ void __launch_bounds__(NT, 3) fused_k(Params p) {
    extern __shared__ __attribute__((aligned(16))) unsigned char lds_[];
    LAS unsigned char* L = (LAS unsigned char*)lds_;
    const unsigned LB = (unsigned)(uintptr_t)L;
    u64 pc0;
    asm volatile("s_getpc_b64 %0" : "=s"(pc0));
    const int b = blockIdx.x, tid = threadIdx.x, l = tid & 63, w = __builtin_amdgcn_readfirstlane(tid >> 6);
    const int g = l >> 4, i16 = l & 15;
    const int x = b & 7, i = b >> 3, isW = i & 1, pi = i >> 1, kc = x * 16 + pi, hh = isW;
    const rsrc_t rs = mkr(p.ws, W_END);
    const unsigned t0 = now32();
    const v4u ones = {SENT, SENT, SENT, SENT};
    STAMP(0);
    if (w >= 8 && !isW) {
        const unsigned ln = (pi * 4 + w - 8) * 320;
        if (ln < CODE_KB * 1024) {
            const u64 ca = (pc0 & ~63ull) + ln;
            unsigned c0, c1, c2, c3, c4;
            asm volatile("s_load_dword %0, %5, 0x0\n\ts_load_dword %1, %5, 0x40\n\ts_load_dword %2, %5, 0x80\n\ts_load_dword %3, %5, 0xc0\n\ts_load_dword %4, %5, 0x100\n\ts_waitcnt lgkmcnt(0)"
                         : "=&s"(c0), "=&s"(c1), "=&s"(c2), "=&s"(c3), "=&s"(c4) : "s"(ca) : "memory");
        }
    }

    if (w >= 8) {
    const int cw = w - 8;
    __builtin_amdgcn_s_setprio(3);
    const v2u ep = ld64(rs, W_EP);
#define CLEAR() { _Pragma("unroll") for (int n = 0; n < NCLR; ++n) st128(rs, l * 16, W_OB + kc * 32768 + cw * 8192 + n * 1024, ones); \
        st128(rs, l < 32 ? l * 16 : OOB, W_LS + kc * 2048 + cw * 512, ones); \
        asm volatile("s_waitcnt vmcnt(0)" ::: "memory"); \
        st64(rs, l == 0 ? 0 : OOB, W_FL + kc * 128 + cw * 8, tag); }
    if (cw == 2) {
        s128(LB + L_MX + l * 16, ones);
        if (l < 36) s128(LB + L_MX + 1024 + l * 16, ones);
    }
    const u64 tag64 = mktag(ep);
    const v2u tag = {(unsigned)tag64, (unsigned)(tag64 >> 32)};
    if (!isW) { wait_until(t0, T_CLR); CLEAR() }
    STAMP(1);
    if (isW) { BAR(); BAR(); }
    {
        const int qb = W_QA + (16 * hh + 4 * cw) * 2048;
        int qv = l * 16;
        unsigned qw = LB + L_QA + 4 * cw * RS + l * 8;
        PIN(qv); PIN(qw);
        wait_until(t0, T_QA);
        STAMP(3);
        v4u q[8];
        unsigned bad;
#define QALOAD _Pragma("unroll") for (int n = 0; n < 8; ++n) q[n] = ld128(rs, qv, qb + n * 1024); \
        bad = 0; _Pragma("unroll") for (int n = 0; n < 8; ++n) { bad |= (q[n][1] ^ tag[1]) | (q[n][3] ^ tag[1]); s64(qw + (n >> 1) * RS + (n & 1) * 512, (v2u){q[n][0], q[n][2]}); }
        POLL(QALOAD, !__all(bad == 0), 4, 28);
    }
    STAMP(4);
    BAR();
    const int vh = 4 * x + (i >> 3), vs = i & 7;
    v4u uvr[4];
    {
        v4u f = ld128(rs, (l >> 1) * 128 + (l & 1) * 16, W_FL + 32 * cw * 128);
        const rsrc_t ru = mkr(p.wuv, 4 << 20);
#pragma unroll
        for (int n = 0; n < 4; ++n) uvr[n] = in128(ru, (l >> 2) * 8192 + (l & 3) * 16, (64 * vs + 16 * n) * 8192 + vh * 256 + cw * 64);
        SCHED();
#define FLBAD !__all((f[0] == tag[0]) & (f[1] == tag[1]) & (f[2] == tag[0]) & (f[3] == tag[1]))
        if (UNLIKELY(FLBAD)) {
            unsigned spins = 0;
            _Pragma("nounroll") do { __builtin_amdgcn_s_sleep(4); FENCE(); f = ld128(rs, (l >> 1) * 128 + (l & 1) * 16, W_FL + 32 * cw * 128); } while ((FLBAD) && ++spins < SPIN_CAP);
        }
        s32(LB + L_OK + cw * 4, 0u);
    }
    STAMP(2);
    __builtin_amdgcn_s_setprio(0);
    auto tail = [&](auto role_) __attribute__((always_inline)) {
    constexpr int ROLE = decltype(role_)::value;
    constexpr bool isV = ROLE == 1, isF = ROLE == 2;
    const int fc = 7 * x + pi - 8;
    v4u wf[16];
    unsigned xb = 0, b1v = 0; v2u b2v = {0, 0};
#define TRANSPOSE8(dst, src) _Pragma("unroll") for (int t = 0; t < 4; ++t) _Pragma("unroll") for (int c = 0; c < 4; ++c) { \
        (dst)[2 * c][t] = perm_lo((src)[2 * t][c], (src)[2 * t + 1][c]); (dst)[2 * c + 1][t] = perm_hi((src)[2 * t][c], (src)[2 * t + 1][c]); }
    if (isV) {
        const rsrc_t ro = mkr(p.wo, 4 << 20);
#pragma unroll
        for (int n = 0; n < 16; ++n) wf[n] = in128(ro, (i16 >> 3) * 32768 + g * 4096 + (i16 & 7) * 16 + ((n & 7) >> 1) * 1024, ((4 * x + cw) * 128 + 64 * (n >> 3) + 16 * (n & 1)) * 1024 + pi * 128);
    } else if (isF) {
        const rsrc_t r1 = mkr(p.w1, 512 * 1792 * 2), r2 = mkr(p.w2, 512 * 1792 * 2);
#pragma unroll
        for (int n = 0; n < 8; ++n) wf[n] = in128(r1, (l >> 2) * 3584 + (l & 3) * 16, (128 * cw + 16 * n) * 3584 + fc * 64);
#pragma unroll
        for (int j = 0; j < 8; ++j) wf[8 + j] = in128(r2, g * 8192 + i16 * 16 + (j & 3) * 1024, (32 * fc + (j & 4)) * 1024 + cw * 256);
        xb = *(const GAS unsigned*)(p.x + 128 * cw + 2 * l);
        b1v = p.b1[32 * fc + 8 * g + (i16 & 7)];
        b2v = *(const GAS v2u*)(p.b2 + 32 * (2 * x + (pi & 1)) + 8 * (l & 3) + 4 * ((l >> 2) & 1));
    }
    SCHED();
    {
        v4u selm;
#pragma unroll
        for (int t = 0; t < 4; ++t) selm[t] = (((l & 7) >> 1) == t) ? ((l & 1) ? 0xffff0000u : 0x0000ffffu) : 0u;
        int lsv = (4 * (l >> 3) + (l & 3)) * 128, obv = (l >> 3) * 512 + (l & 7) * 16;
        const int lss = W_LS + ((vh >> 4) * 128 + 32 * cw) * 128 + (vh & 15) * 8, obs = W_OB + (vh * 8 + vs) * 16384 + cw * 4096;
        unsigned wp = LB + L_SC + ((g >> 1) == (i16 >> 3) ? 2 * cw + (g >> 1) : 10 + cw) * 144 + (8 * (i16 & 7) + 4 * (g & 1)) * 2;
        unsigned rp = LB + L_SC + (i16 & 7) * 144 + 16 * g;
        unsigned sp = LB + L_SC + 1280 + cw * 4, sq = LB + L_SC + 1280;
        int cxv = l < 16 ? l * 4 : OOB;
        PIN(selm); PIN(lsv); PIN(obv); PIN(wp); PIN(rp); PIN(sp); PIN(sq); PIN(cxv);
        SCHED();
        STAMP(5);
        BAR();
        const unsigned tB = now32();
        v4u uvf[2][2];
        {
            const unsigned ub = LB + L_CT + cw * 8192;
#pragma unroll
            for (int n = 0; n < 4; ++n) s128(ub + (16 * n + (l >> 2)) * 80 + (l & 3) * 16, uvr[n]);
            const unsigned ur = ub + (16 * (i16 >> 2) + g) * 80 + (i16 & 3) * 8;
#pragma unroll
            for (int tl = 0; tl < 2; ++tl)
#pragma unroll
                for (int ks = 0; ks < 2; ++ks) {
                    const v2u a_ = ltr(ur + (4 * ks) * 80 + tl * 32), c_ = ltr(ur + (4 * ks + 8) * 80 + tl * 32);
                    uvf[tl][ks] = (v4u){a_[0], a_[1], c_[0], c_[1]};
                }
            PIN(uvf[0][0]); PIN(uvf[0][1]); PIN(uvf[1][0]); PIN(uvf[1][1]);
        }
        if (isV) {
#pragma unroll
            for (int hb = 0; hb < 2; ++hb) {
                v4u r[8];
#pragma unroll
                for (int n = 0; n < 8; ++n) r[n] = wf[8 * hb + n];
                TRANSPOSE8(wf + 8 * hb, r)
#pragma unroll
                for (int n = 0; n < 8; ++n) PIN(wf[8 * hb + n]);
                SCHED();
            }
        }
        SCHED();
        wait_until(tB, DLY_B);
        STAMP(10);
        f32x4 acc;
        unsigned mx;
#define V1TRY { const v2u ml = ld64(rs, lsv, lss); v4u od[4]; \
            _Pragma("unroll") for (int n = 0; n < 4; ++n) od[n] = ld128(rs, obv + n * 128, obs); \
            SCHED(); \
            const float m = __uint_as_float(ml[0]); \
            float M = fmx3(m, dpp<0xB1>(m), dpp<0x4E>(m)); \
            M = fmx(M, dpp<0xB1>(M)); \
            M = x32_max(x16_max(fmx(M, dpp<0x128>(M)))); \
            const float e = __builtin_amdgcn_exp2f(m - M); \
            const unsigned dup = pk(e, e); \
            const v4u a0 = sel4(dppu<0x00>(dup), selm), a1 = sel4(dppu<0x55>(dup), selm), a2 = sel4(dppu<0xAA>(dup), selm), a3 = sel4(dppu<0xFF>(dup), selm); \
            float Lw = bflo(dup) * __uint_as_float(ml[1]); \
            Lw += dpp<0xB1>(Lw); Lw += dpp<0x4E>(Lw); Lw += dpp<0x128>(Lw); \
            Lw = x32_sum(x16_sum(Lw)); \
            s32f(sp, M); s32f(sp + 16, Lw); \
            mx = max(ml[0], ml[1]); \
            SCHED(); \
            acc = mfma(a0, od[0], (f32x4){0.f, 0.f, 0.f, 0.f}); acc = mfma(a1, od[1], acc); acc = mfma(a2, od[2], acc); acc = mfma(a3, od[3], acc); \
            _Pragma("unroll") for (int n = 0; n < 4; ++n) mx = max(mx, umax4(od[n])); }
        POLL(V1TRY, !__all(mx != SENT), 1, 0);
        STAMP(6);
        s64(wp, (v2u){pk(acc[0], acc[1]), pk(acc[2], acc[3])});
        BAR();
        const v4u a0 = l128(rp), a1 = l128(rp + 64);
        const f32x4 Ms = asf(l128(sq)), Ls = asf(l128(sq + 16));
        f32x4 d0 = {0.f, 0.f, 0.f, 0.f}, d1 = {0.f, 0.f, 0.f, 0.f};
        d0 = mfma(a0, uvf[0][0], d0); d1 = mfma(a0, uvf[1][0], d1);
        d0 = mfma(a1, uvf[0][1], d0); d1 = mfma(a1, uvf[1][1], d1);
        const float Mg = max4(Ms);
        const f32x4 fw = {__builtin_amdgcn_exp2f(Ms[0] - Mg), __builtin_amdgcn_exp2f(Ms[1] - Mg), __builtin_amdgcn_exp2f(Ms[2] - Mg), __builtin_amdgcn_exp2f(Ms[3] - Mg)};
        const float Lr = __builtin_amdgcn_rcpf(sum4(fw * Ls));
        const float fa = ((g & 1) ? fw[2] : fw[0]) * Lr, fb = ((g & 1) ? fw[3] : fw[1]) * Lr;
        const float r0 = x16_sum(fa * (d0[0] + d0[1]) + fb * (d0[2] + d0[3])), r1 = x16_sum(fa * (d1[0] + d1[1]) + fb * (d1[2] + d1[3]));
        st32(rs, cxv, W_CX + (vh * 8 + vs) * 256 + cw * 64, san(pk(r0, r1)));
    }
    const unsigned tV = now32();
    STAMP(7);
    if (isV) {
        const int cbs = W_CX + (4 * x + cw) * 2048;
        int cbv = l * 16, opv = l < 16 ? l * 16 : OOB;
        unsigned wp = LB + L_CT + cw * 4096 + (l >> 4) * 272 + i16 * 16;
        unsigned rp = LB + L_CT + cw * 4096 + (i16 & 7) * 272 + (i16 >> 3) * 64 + g * 16;
        unsigned ow = LB + L_QA + ((g >> 1) == (i16 >> 3) ? 2 * cw + (g >> 1) : 8 + cw) * 272 + (i16 & 7) * 32 + (g & 1) * 16, orr = LB + L_QA + g * 544 + i16 * 16;
        PIN(cbv); PIN(opv); PIN(wp); PIN(rp); PIN(ow); PIN(orr);
#pragma unroll
        for (int t = 0; t < 16; ++t) PIN(wf[t]);
        SCHED();
        wait_until(tV, DLY_V);
        v4u c0, c1;
        POLL(c0 = ld128(rs, cbv, cbs); c1 = ld128(rs, cbv + 1024, cbs), !__all(max(umax4(c0), umax4(c1)) != SENT), 1, 8);
        STAMP(8);
        s128(wp, c0); s128(wp + 4 * 272, c1);
        const v4u cx0 = l128(rp), cx1 = l128(rp + 128);
        f32x4 d[8];
#pragma unroll
        for (int t = 0; t < 8; ++t) d[t] = mfma(cx0, wf[t], (f32x4){0.f, 0.f, 0.f, 0.f});
#pragma unroll
        for (int t = 0; t < 8; ++t) d[t] = mfma(cx1, wf[8 + t], d[t]);
        v4u e;
#pragma unroll
        for (int t = 0; t < 4; ++t) {
            auto s = __builtin_amdgcn_permlane16_swap(__float_as_uint(sum4(d[t])), __float_as_uint(sum4(d[t + 4])), false, false);
            e[t] = __float_as_uint(__uint_as_float(s[0]) + __uint_as_float(s[1]));
        }
        s128(ow, e);
        BAR();
        if (cw == 0) {
            const f32x4 s = asf(l128(orr)) + asf(l128(orr + 272));
            float s0 = s[0], s1 = s[1], s2 = s[2], s3 = s[3];
            red2_rows(s0, s1); red2_rows(s2, s3);
            st128(rs, opv, W_OP + x * 2048 + pi * 256, (v4u){san(__float_as_uint(s0)), san(__float_as_uint(s1)), san(__float_as_uint(s2)), san(__float_as_uint(s3))});
        }
        STAMP(13);
        STAMP_OUT();
        return;
    }
    if (!isF) { STAMP_OUT(); return; }
    {
        const unsigned ub = LB + L_CT + 32768 + cw * 10240;
#pragma unroll
        for (int n = 0; n < 8; ++n) s128(ub + (16 * n + (l >> 2)) * 80 + (l & 3) * 16, wf[n]);
        const unsigned ur = ub + (8 * g + (i16 >> 2)) * 80 + (i16 & 3) * 8;
#pragma unroll
        for (int tl = 0; tl < 2; ++tl)
#pragma unroll
            for (int ks = 0; ks < 4; ++ks) {
                const v2u a_ = ltr(ur + (32 * ks) * 80 + tl * 32), c_ = ltr(ur + (32 * ks + 4) * 80 + tl * 32);
                wf[4 * tl + ks] = (v4u){a_[0], a_[1], c_[0], c_[1]};
            }
        v4u q[8];
#pragma unroll
        for (int n = 0; n < 8; ++n) q[n] = wf[8 + n];
        TRANSPOSE8(wf + 8, q)
    }
    {
        const v4u one = {0x3f803f80u, 0x3f803f80u, 0x3f803f80u, 0x3f803f80u};
        f32x4 d0 = {0.f, 0.f, 0.f, 0.f}, d1 = {0.f, 0.f, 0.f, 0.f};
#pragma unroll
        for (int ks = 0; ks < 4; ++ks) { d0 = mfma(one, wf[ks], d0); d1 = mfma(one, wf[4 + ks], d1); }
        s32f(LB + L_SC + 2560 + i16 * 16 + cw * 4, d0[0]); s32f(LB + L_SC + 2560 + (16 + i16) * 16 + cw * 4, d1[0]);
    }
    {
        int opv = l * 8;
        unsigned hw = LB + L_P + cw * 544 + l * 4, hr = LB + L_P + cw * 544 + (i16 & 1) * 272 + g * 16;
        unsigned hf = LB + L_QA + (128 * cw + 2 * l) * 4;
        unsigned uw = LB + L_SC + 3072 + i16 * 16 + cw * 4, ur = LB + L_SC + 2560 + (8 * g + (i16 & 7)) * 16;
        unsigned sw = LB + L_SC + 3584 + cw * 4, sr = LB + L_SC + 3584;
        int ypv = (g & 1) ? OOB : (i16 >> 2) * (56 * 128) + (i16 & 3) * 16 + (g >> 1) * 64;
        float x0 = bflo(xb), x1 = bfhi(xb), b1f = bflo(b1v);
        PIN(opv); PIN(hw); PIN(hr); PIN(hf); PIN(uw); PIN(ur); PIN(sw); PIN(sr); PIN(ypv); PIN(x0); PIN(x1); PIN(b1f);
#pragma unroll
        for (int t = 0; t < 16; ++t) PIN(wf[t]);
        SCHED();
        wait_until(tV, DLY_C);
        v2u op[8];
        unsigned mx;
#define OPLOAD _Pragma("unroll") for (int n = 0; n < 8; ++n) op[n] = ld64(rs, opv, W_OP + n * 2048 + cw * 512); \
        mx = 0; _Pragma("unroll") for (int n = 0; n < 8; ++n) mx = max(mx, max(op[n][0], op[n][1]));
        POLL(OPLOAD, !__all(mx != SENT), 1, 8);
        STAMP(8);
        f32x2 h = {x0, x1};
#pragma unroll
        for (int n = 0; n < 8; ++n) h += __builtin_bit_cast(f32x2, op[n]);
        const float mw = wave_sum(h[0] + h[1]) * (1.f / 128.f);
        const float c0 = h[0] - mw, c1 = h[1] - mw;
        const unsigned hi = pk(c0, c1), lo = pk(c0 - bflo(hi), c1 - bfhi(hi));
        s32(hw, hi); s32(hw + 272, lo);
        v4u a[4];
#pragma unroll
        for (int ks = 0; ks < 4; ++ks) a[ks] = l128v(hr + ks * 64);
        s64(hf, __builtin_bit_cast(v2u, h));
        SCHED();
        const float m2 = wave_sum(c0 * c0 + c1 * c1);
        f32x4 d0 = {0.f, 0.f, 0.f, 0.f}, d1 = {0.f, 0.f, 0.f, 0.f};
#pragma unroll
        for (int ks = 0; ks < 4; ++ks) { d0 = mfma(a[ks], wf[ks], d0); d1 = mfma(a[ks], wf[4 + ks], d1); }
        s32f(sw, mw); s32f(sw + 16, m2);
        s32f(uw, d0[0] + d0[1]); s32f(uw + 256, d1[0] + d1[1]);
        BAR();
        const f32x4 mm = asf(l128(sr)), qq = asf(l128(sr + 16));
        const f32x4 cc = asf(l128(ur)), uu = asf(l128(ur + 512));
        const float mean = sum4(mm) * 0.25f;
        const f32x4 dm = mm - mean;
        const float var = (sum4(qq) + 128.f * sum4(dm * dm)) * (1.f / 512.f);
        const float rstd = __builtin_amdgcn_rsqf(var + 1e-5f);
        const float u = (sum4(uu) + sum4(dm * cc)) * rstd + b1f;
        const float z = u * (1.f + 0.044715f * u * u) * (-2.f * 0.7978845608028654f * 1.4426950408889634f);
        const float ge = u * __builtin_amdgcn_rcpf(1.f + __builtin_amdgcn_exp2f(z));
        const unsigned pr = pk(ge, dpp<0x101>(ge));
        const v4u fa = {pr, dppu<0x102>(pr), dppu<0x104>(pr), dppu<0x106>(pr)};
        float y[8];
#pragma unroll
        for (int t = 0; t < 8; ++t) { f32x4 d = {0.f, 0.f, 0.f, 0.f}; d = mfma(fa, wf[8 + t], d); y[t] = d[0]; }
        v4u v;
#pragma unroll
        for (int t = 0; t < 4; ++t) v[t] = san(__float_as_uint(g ? y[4 + t] : y[t]));
        st128(rs, ypv, W_YP + (4 * cw * 56 + fc) * 128, v);
    }
    const unsigned tF = now32();
    STAMP(13);
    if (pi < 10 && cw == 0) {
        const int fin = 2 * x + (pi & 1);
        const int col = 32 * fin + 8 * (l & 3) + 4 * ((l >> 2) & 1);
        f32x4 hv = asf(l128(LB + L_QA + col * 4));
        hv += (f32x4){bflo(b2v[0]), bfhi(b2v[0]), bflo(b2v[1]), bfhi(b2v[1])};
        const rsrc_t rout = mkr(p.out, 1024);
        int yv_ = l * 16, ov = l < 8 ? col * 2 : OOB;
        PIN(hv); PIN(yv_); PIN(ov);
        SCHED();
        wait_until(tF, DLY_D);
        v4u yv[7];
        unsigned mx;
#define YLOAD _Pragma("unroll") for (int n = 0; n < 7; ++n) yv[n] = ld128(rs, yv_, W_YP + fin * 56 * 128 + n * 1024); \
        mx = 0; _Pragma("unroll") for (int n = 0; n < 7; ++n) mx = max(mx, umax4(yv[n]));
        POLL(YLOAD, !__all(mx != SENT), 1, 16);
        STAMP(12);
        f32x4 s = asf(yv[0]);
#pragma unroll
        for (int n = 1; n < 7; ++n) s += asf(yv[n]);
        float s0 = s[0], s1 = s[1], s2 = s[2], s3 = s[3];
        red2_rows(s0, s1); red2_rows(s2, s3);
        s0 += dpp<0x128>(s0); s1 += dpp<0x128>(s1); s2 += dpp<0x128>(s2); s3 += dpp<0x128>(s3);
        __builtin_amdgcn_raw_buffer_store_b64((v2u){pk(hv[0] + s0, hv[1] + s1), pk(hv[2] + s2, hv[3] + s3)}, rout, ov, 0, 0);
        if (fin == 0) st64(rs, l == 0 ? 0 : OOB, W_EP, tag);
    }
    STAMP_OUT();
    };
    if (!isW && pi < 8) { tail(IC<1>{}); __builtin_amdgcn_endpgm(); }
    if (!isW && pi < 15) { tail(IC<2>{}); __builtin_amdgcn_endpgm(); }
    tail(IC<0>{});
    __builtin_amdgcn_endpgm();
    }
    {
        const int hq = x * 4 + (i >> 3), cq = (i & 7) >> 1;
        unsigned tag32 = 0;
#define UKDMA() { const rsrc_t rk = mkr(p.wuk, 4 << 20); _Pragma("unroll") for (int n = 0; n < 4; ++n) \
            __builtin_amdgcn_raw_ptr_buffer_load_lds(rk, (LAS void*)(L + L_CT + 16 * w * RS + n * 1024), 16, g * 8192 + ((i16 ^ (4 * n + g)) * 16), \
                                                     hq * 256 + (128 * cq + 16 * w + 4 * n) * 8192, 0, 0); }
        if (__builtin_expect(w < 2, 1)) {
            const rsrc_t rq = mkr(p.wq, 4 << 20), rx = mkr(p.x, 1024);
            const v4u xs = in128(rx, l * 16, 0);
            const v4u xr = in128(rx, g * 16, (i & 7) * 128 + w * 64);
            const v2u ep = ld64(rs, W_EP);
            SCHED();
            v4u wq[8];
#pragma unroll
            for (int n = 0; n < 8; ++n) wq[n] = in128(rq, g * 65536 + i16 * 16, hq * 256 + (64 * (i & 7) + 32 * w + n) * 8192);
            SCHED();
            v4u selm;
#pragma unroll
            for (int t = 0; t < 4; ++t) selm[t] = (((l & 7) >> 1) == t) ? ((l & 1) ? 0xffff0000u : 0x0000ffffu) : 0u;
            int qpv = l < 32 ? l * 16 : OOB;
            float xv[8];
#pragma unroll
            for (int t = 0; t < 4; ++t) { xv[2 * t] = bflo(xs[t]); xv[2 * t + 1] = bfhi(xs[t]); }
            float sm = 0.f;
#pragma unroll
            for (int t = 0; t < 8; ++t) sm += xv[t];
            const float mean = wave_sum(sm) * (1.f / 512.f);
            float sq = 0.f;
#pragma unroll
            for (int t = 0; t < 8; ++t) { const float d = xv[t] - mean; sq += d * d; }
            const float rstd = __builtin_amdgcn_rsqf(wave_sum(sq) * (1.f / 512.f) + 1e-5f);
            v4u xa[8];
#pragma unroll
            for (int n = 0; n < 8; ++n) {
                const float xn = (((n & 1) ? bfhi(xr[n >> 1]) : bflo(xr[n >> 1])) - mean) * rstd;
                xa[n] = sel4(pk(xn, xn), selm);
            }
            tag32 = (unsigned)(mktag(ep) >> 32);
            PIN(qpv);
            SCHED();
            f32x4 acc = {0.f, 0.f, 0.f, 0.f};
#pragma unroll
            for (int n = 0; n < 8; ++n) acc = mfma(xa[n], wq[n], acc);
            st128(rs, qpv, W_QP + (hq * 16 + (i & 7) * 2 + w) * 512, (v4u){pk(acc[0], acc[1]), tag32, pk(acc[2], acc[3]), tag32});
            STAMP(1);
        }
        if (isW) {
            UKDMA()
            if (w >= 2) { const v2u ep = ld64(rs, W_EP); tag32 = (unsigned)(mktag(ep) >> 32); }
            const int kr = 4 * (i16 & 3) + (i16 >> 2);
            unsigned wp = LB + L_SC + (2 * w + (l >> 5)) * 272 + i16 * 16 + (g & 1) * 8;
            unsigned rp = LB + L_SC + i16 * 272 + 16 * g;
            unsigned kp = LB + L_CT + 16 * w * RS + kr * 256;
            int qv = l * 16, qav = l < 4 ? l * 16 : OOB;
            PIN(wp); PIN(rp); PIN(qv); PIN(qav); PIN(kp);
            SCHED();
            wait_until(t0, T_QP);
            v4u qg;
            POLL(qg = ld128(rs, qv, W_QP + (hq * 16 + 2 * w) * 512), !__all((qg[1] == tag32) & (qg[3] == tag32)), 1, 24);
            s64(wp, (v2u){qg[0], qg[2]});
            v4u uk[4];
#pragma unroll
            for (int s = 0; s < 4; ++s) uk[s] = l128v(kp + (((4 * s + g) ^ kr) * 16));
            STAMP(9);
            BAR();
            v4u a[4];
#pragma unroll
            for (int s = 0; s < 4; ++s) a[s] = l128(rp + 64 * s);
            f32x4 d0 = {0.f, 0.f, 0.f, 0.f};
#pragma unroll
            for (int s = 0; s < 4; ++s) d0 = mfma(a[s], uk[s], d0);
            constexpr float QS = 0.08838834764831845f * 1.4426950408889634f;
            const float r0 = x32_sum(x16_sum(sum4(d0))) * QS;
            const unsigned pr = pk(r0, dpp<0x104>(r0)), pr2 = dppu<0x108>(pr);
            st128(rs, qav, W_QA + hq * 2048 + cq * 512 + w * 64, (v4u){pr, tag32, pr2, tag32});
            STAMP(2);
            BAR();
        } else {
            wait_until(t0, T_C);
        }
        const int wk = w;
        {
            const rsrc_t rc = mkr(p.ckv, 16 << 20);
#pragma unroll
            for (int r = 0; r < 16; ++r)
                __builtin_amdgcn_raw_ptr_buffer_load_lds(rc, (LAS void*)(L + L_CT + (16 * w + r) * RS), 16, l * 16, (kc * 128 + 16 * w + r) * 1024, 0, 0);
        }
        const int rho = 8 * (i16 >> 3) + 2 * (i16 & 3) + ((i16 >> 2) & 1);
        unsigned qp = LB + L_QA + i16 * RS + g * 256;
        unsigned cp = LB + L_CT + (16 * wk + rho) * RS + g * 256;
        unsigned tp0 = LB + L_CT + (8 * g + 2 * (i16 >> 2)) * RS + 128 * w + 8 * (i16 & 3);
        unsigned tp1 = tp0 + 64 * RS;
        unsigned mo = LB + L_MX + i16 * 48 + (g >> 1) * 16, mx_ = LB + L_MX + i16 * 48 + (1 - (g >> 1)) * 16;
        unsigned mq = LB + L_MX + i16 * 48 + (wk & 1) * 16 + (wk >> 1) * 4;
        unsigned pw = LB + L_P + i16 * PS + (16 * wk + 4 * g) * 2;
        unsigned pr = LB + L_P + i16 * PS + g * 16;
        int obv = (4 * g + 2 * (i16 >> 3)) * (8 * 16384) + (i16 & 7) * 16;
        unsigned okp = LB + L_OK;
        PIN(qp); PIN(cp); PIN(tp0); PIN(tp1); PIN(mo); PIN(mx_); PIN(mq); PIN(pw); PIN(pr); PIN(obv); PIN(okp);
        STAMP(3);
        BAR();
        STAMP(4);
        v4u qf[16], cr[16];
#pragma unroll
        for (int s = 0; s < 16; ++s) qf[s] = l128(qp + s * 16);
        SCHED();
        __builtin_amdgcn_s_waitcnt(0x0F70);
        FENCE();
        STAMP(5);
#pragma unroll
        for (int s = 0; s < 16; ++s) cr[s] = l128(cp + s * 16);
        SCHED();
        f32x4 sc = {0.f, 0.f, 0.f, 0.f};
#pragma unroll
        for (int s = 0; s < 16; ++s) sc = mfma(cr[s], qf[s], sc);
        const float mw = __builtin_ceilf(x32_max(x16_max(max4(sc))));
        {
            const unsigned p0 = pk(__builtin_amdgcn_exp2f(sc[0] - mw), __builtin_amdgcn_exp2f(sc[1] - mw)), p1 = pk(__builtin_amdgcn_exp2f(sc[2] - mw), __builtin_amdgcn_exp2f(sc[3] - mw));
            s64(pw, (v2u){p0, p1});
            const bf16x2 one2 = __builtin_bit_cast(bf16x2, 0x3f803f80u);
            float ls = __builtin_amdgcn_fdot2_f32_bf16(__builtin_bit_cast(bf16x2, p0), one2, 0.f, false);
            ls = __builtin_amdgcn_fdot2_f32_bf16(__builtin_bit_cast(bf16x2, p1), one2, ls, false);
            s32(mq, san(__float_as_uint(mw)));
            ls = x32_sum(x16_sum(ls));
            s32(mq + 768, san(__float_as_uint(ls)));
        }
        STAMP(6);
        v4u m0, m1, pf[4];
#define MXLOAD m0 = l128v(mo); m1 = l128v(mx_); _Pragma("unroll") for (int ks = 0; ks < 4; ++ks) pf[ks] = l128v(pr + ks * 64)
        POLL(MXLOAD, !__all(max(umax4(m0), umax4(m1)) != SENT), 1, 0);
        FENCE();
        STAMP(7);
        v4u cf[4][4];
#define TRREAD(ks) _Pragma("unroll") for (int ct = 0; ct < 4; ++ct) { \
            const unsigned t_ = ((ks) < 2 ? tp0 : tp1) + (32 * ((ks) & 1)) * RS + 32 * ct; \
            const v2u a_ = ltr(t_), c_ = ltr(t_ + RS); \
            cf[ks][ct] = (v4u){a_[0], a_[1], c_[0], c_[1]}; }
        TRREAD(0) TRREAD(1)
        v4u okw = l128v(okp);
        SCHED();
        const float M = fmx(max4(asf(m0)), max4(asf(m1)));
#pragma unroll
        for (int ks = 0; ks < 4; ++ks) {
            const unsigned short dl = (unsigned short)(unsigned)__builtin_fminf((M - __uint_as_float(m0[ks])) * 128.f, 32640.f);
#pragma unroll
            for (int t = 0; t < 4; ++t) pf[ks][t] = pksubsat(pf[ks][t], dl);
        }
        SCHED();
        TRREAD(2) TRREAD(3)
        SCHED();
        f32x4 o[4];
#pragma unroll
        for (int ct = 0; ct < 4; ++ct) o[ct] = (f32x4){0.f, 0.f, 0.f, 0.f};
#pragma unroll
        for (int ks = 0; ks < 2; ++ks)
#pragma unroll
            for (int ct = 0; ct < 4; ++ct) o[ct] = mfma(pf[ks], cf[ks][ct], o[ct]);
        SCHED();
        if (UNLIKELY(__builtin_amdgcn_readfirstlane(umax4(okw)) == SENT)) {
            unsigned spins = 0;
            _Pragma("nounroll") do { __builtin_amdgcn_s_sleep(1); okw = l128v(okp); } while (__builtin_amdgcn_readfirstlane(umax4(okw)) == SENT && ++spins < SPIN_CAP);
        }
        FENCE();
        SCHED();
#pragma unroll
        for (int ks = 2; ks < 4; ++ks)
#pragma unroll
            for (int ct = 0; ct < 4; ++ct) o[ct] = mfma(pf[ks], cf[ks][ct], o[ct]);
        STAMP(11);
        const int obs = W_OB + (16 * hh * 8 + w) * 16384 + kc * 128;
#pragma unroll
        for (int r = 0; r < 2; ++r) {
            v4u v;
#pragma unroll
            for (int t = 0; t < 2; ++t) {
                const unsigned A = san(pk(o[2 * t][r], o[2 * t + 1][r])), B = san(pk(o[2 * t][r + 2], o[2 * t + 1][r + 2]));
                v[t] = (unsigned)__builtin_amdgcn_update_dpp((int)A, (int)B, 0x128, 0xf, 0xC, false);
                v[2 + t] = (unsigned)__builtin_amdgcn_update_dpp((int)B, (int)A, 0x128, 0xf, 0x3, false);
            }
            st128(rs, obv, obs + r * (8 * 16384), v);
        }
        if (w == 0) {
            v4u l0, l1;
            POLL(l0 = l128v(mo + 768); l1 = l128v(mx_ + 768), !__all(max(umax4(l0), umax4(l1)) != SENT), 1, 0);
            float lt = 0.f;
#pragma unroll
            for (int t = 0; t < 4; ++t) lt += __uint_as_float(l0[t]) * __builtin_amdgcn_exp2f(__uint_as_float(m0[t]) - M) + __uint_as_float(l1[t]) * __builtin_amdgcn_exp2f(__uint_as_float(m1[t]) - M);
            st64(rs, l < 16 ? l * 8 : OOB, W_LS + (hh * 128 + kc) * 128, (v2u){san(__float_as_uint(M)), san(__float_as_uint(lt))});
        }
        STAMP(8);
        STAMP_OUT();
    }
}

#define PMODE 4
#define PAMP 30
__global__ void __launch_bounds__(512) probe_k(Params p) {
    extern __shared__ __attribute__((aligned(16))) unsigned char lds_[];
    LAS unsigned char* L = (LAS unsigned char*)lds_;
    u64 t0; asm volatile("s_memrealtime %0\n\ts_waitcnt lgkmcnt(0)" : "=s"(t0) :: "memory");
    __builtin_amdgcn_sched_barrier(0);
    const int b = blockIdx.x, l = threadIdx.x & 63, w = __builtin_amdgcn_readfirstlane(threadIdx.x >> 6);
    const int x = b & 7, i = b >> 3, kc = x * 16 + (i >> 1);
    if (PMODE == 1 || PMODE == 2) {
        const rsrc_t rc = mkr(p.ckv, 16 << 20);
#pragma unroll
        for (int r = 0; r < 16; ++r)
            __builtin_amdgcn_raw_ptr_buffer_load_lds(rc, (LAS void*)(L + (16 * w + r) * RS), 16, l * 16, (kc * 128 + 16 * w + r) * 1024, 0, PMODE == 2 ? 2 : 0);
    } else if (PMODE == 3) {
        const rsrc_t rq = mkr(p.wq, 4 << 20);
        if (w < 2) {
#pragma unroll
        for (int n = 0; n < 8; ++n)
            __builtin_amdgcn_raw_ptr_buffer_load_lds(rq, (LAS void*)(L + (8 * w + n) * 1024), 16, (l >> 4) * 65536 + (l & 15) * 16, (x * 4 + (i >> 3)) * 256 + (64 * (i & 7) + 32 * w + n) * 8192, 0, 0);
        }
    } else if (PMODE == 4) {
        const rsrc_t rx = mkr(p.x, 1024);
        __builtin_amdgcn_raw_ptr_buffer_load_lds(rx, (LAS void*)(L + w * 1024), 16, l * 16, 0, 0, 0);
    } else if (PMODE == 5 || PMODE == 6) {
        const rsrc_t rs = mkr(p.ws - (PMODE == 6 ? (200u << 20) : 0), W_END);
        const v4u ones = {SENT, SENT, SENT, SENT};
        st128(rs, l * 16, W_OB + b * 16384 + w * 2048, ones); st128(rs, l * 16, W_OB + b * 16384 + w * 2048 + 1024, ones);
    } else if (PMODE == 7) {
        asm volatile("" :: "s"(p.x));
    } else if (PMODE == 8) {
        const rsrc_t rc = mkr(p.ckv, 16 << 20), r1 = mkr(p.wq, 4 << 20), r2 = mkr(p.wuk, 4 << 20), r3 = mkr(p.wuv, 4 << 20), r4 = mkr(p.wo, 4 << 20);
#pragma unroll
        for (int r = 0; r < 2; ++r) {
            __builtin_amdgcn_raw_ptr_buffer_load_lds(r1, (LAS void*)(L + (16 * w + r) * RS), 16, l * 16, b * 16384 + w * 2048 + r * 1024, 0, 0);
            __builtin_amdgcn_raw_ptr_buffer_load_lds(r2, (LAS void*)(L + (16 * w + 2 + r) * RS), 16, l * 16, b * 16384 + w * 2048 + r * 1024, 0, 0);
            __builtin_amdgcn_raw_ptr_buffer_load_lds(r3, (LAS void*)(L + (16 * w + 4 + r) * RS), 16, l * 16, b * 16384 + w * 2048 + r * 1024, 0, 0);
            __builtin_amdgcn_raw_ptr_buffer_load_lds(r4, (LAS void*)(L + (16 * w + 6 + r) * RS), 16, l * 16, b * 16384 + w * 2048 + r * 1024, 0, 0);
        }
#pragma unroll
        for (int r = 0; r < 8; ++r)
            __builtin_amdgcn_raw_ptr_buffer_load_lds(rc, (LAS void*)(L + (16 * w + 8 + r) * RS), 16, l * 16, (b * 64 + 8 * w + r) * 1024, 0, 0);
    }
    asm volatile("s_waitcnt vmcnt(0)" ::: "memory");
    __builtin_amdgcn_sched_barrier(0);
    u64 t1; asm volatile("s_memrealtime %0\n\ts_waitcnt lgkmcnt(0)" : "=s"(t1) :: "memory");
    const u64 until = t1 + PAMP * (t1 - t0);
    while (__builtin_amdgcn_s_memrealtime() < until) __builtin_amdgcn_s_sleep(2);
}

extern "C" void kernel_launch(void* const* d_in, const int* in_sizes, int n_in, void* d_out, int out_size, void* d_ws, size_t ws_size,
                              hipStream_t stream) {
    Params p;
    p.x = (const u16*)d_in[0]; p.ckv = (const u16*)d_in[1]; p.wq = (const u16*)d_in[2]; p.wuk = (const u16*)d_in[3];
    p.wuv = (const u16*)d_in[4]; p.wo = (const u16*)d_in[5]; p.w1 = (const u16*)d_in[7]; p.b1 = (const u16*)d_in[8];
    p.w2 = (const u16*)d_in[9]; p.b2 = (const u16*)d_in[10]; p.out = (u16*)d_out; p.ws = (unsigned char*)d_ws + WS_OFF(ws_size);
    (void)hipFuncSetAttribute((const void*)fused_k, hipFuncAttributeMaxDynamicSharedMemorySize, L_END);
    (void)hipFuncSetAttribute((const void*)probe_k, hipFuncAttributeMaxDynamicSharedMemorySize, L_END);
    hipLaunchKernelGGL(probe_k, dim3(NWG), dim3(512), L_END, stream, p);
    hipLaunchKernelGGL(fused_k, dim3(NWG), dim3(NT), L_END, stream, p);
}
```

```cpp
#include <hip/hip_runtime.h>
#include <stdint.h>

typedef unsigned short u16;
typedef unsigned long long u64;
typedef unsigned v4u __attribute__((ext_vector_type(4)));
typedef unsigned v2u __attribute__((ext_vector_type(2)));
typedef float f32x4 __attribute__((ext_vector_type(4)));
typedef float f32x2 __attribute__((ext_vector_type(2)));
typedef __bf16 bf16x8 __attribute__((ext_vector_type(8)));
typedef __bf16 bf16x2 __attribute__((ext_vector_type(2)));
typedef short s16x4 __attribute__((ext_vector_type(4)));
#define GAS __attribute__((address_space(1)))
#define LAS __attribute__((address_space(3)))
#define DI __device__ __forceinline__

#define STAMPS 0
#define KNOB(i, v) (v)
#define T_C KNOB(0, 130)
#define T_CLR KNOB(1, 130)
#define T_QP KNOB(2, 170)
#define T_QA KNOB(3, 400)
#define DLY_B KNOB(4, 0)
#define DLY_V KNOB(5, 15)
#define DLY_C KNOB(6, 100)
#define DLY_D KNOB(7, 15)
#define NCLR 8
#define WS_TAIL 1
#define WS_OFF(sz) (WS_TAIL ? (((sz) - (16u << 20)) & ~(size_t)((2u << 20) - 1)) : 0)
#define CODE_KB 19

constexpr int NWG = 256, NT = 768;
constexpr int W_EP = 0, W_FL = 4096, W_QA = 65536, W_LS = W_QA + 65536, W_CX = W_LS + 32768, W_OP = W_CX + 65536,
              W_YP = W_OP + 16384, W_SMALL_END = W_LS + 128 * 2048, W_QP = W_SMALL_END, W_OB = 1 << 20, W_END = W_OB + (4 << 20), W_ST = 8 << 20;
static_assert(W_YP + 16 * 56 * 128 <= W_SMALL_END && W_QP + 262144 <= W_OB, "ws map");
constexpr int RS = 1040, L_CT = 0, L_QA = 128 * RS, L_P = L_QA + 16 * RS, PS = 272, L_MX = L_P + 16 * PS, L_LS = L_MX + 768,
              L_OK = L_LS + 768, L_CN = L_OK + 16, L_SC = L_OK + 64, L_END = L_SC + 4352;
constexpr unsigned SENT = 0xFFFFFFFFu;
constexpr unsigned SPIN_CAP = 200000;

struct Params {
    const u16 *x, *ckv, *wq, *wuk, *wuv, *wo, *w1, *b1, *w2, *b2;
    u16* out;
    unsigned char* ws;
};

DI float bflo(unsigned u) { return __uint_as_float(u << 16); }
DI float bfhi(unsigned u) { return __uint_as_float(u & 0xffff0000u); }
DI unsigned pk(float a, float b) { f32x2 f = {a, b}; return __builtin_bit_cast(unsigned, __builtin_convertvector(f, bf16x2)); }
DI unsigned san(unsigned u) { return u < 0xFFFFFFFEu ? u : 0xFFFFFFFEu; }
DI f32x4 mfma(v4u a, v4u b, f32x4 c) { return __builtin_amdgcn_mfma_f32_16x16x32_bf16(__builtin_bit_cast(bf16x8, a), __builtin_bit_cast(bf16x8, b), c, 0, 0, 0); }
template <int CTRL> DI float dpp(float v) { return __int_as_float(__builtin_amdgcn_update_dpp(0, __float_as_int(v), CTRL, 0xf, 0xf, true)); }
template <int CTRL> DI unsigned dppu(unsigned v) { return (unsigned)__builtin_amdgcn_update_dpp(0, (int)v, CTRL, 0xf, 0xf, true); }
DI float fmx(float a, float b) { return __builtin_elementwise_maximum(a, b); }
DI float fmx3(float a, float b, float c) { return fmx(fmx(a, b), c); }
DI float x16_sum(float v) { auto r = __builtin_amdgcn_permlane16_swap(__float_as_uint(v), __float_as_uint(v), false, false); return __uint_as_float(r[0]) + __uint_as_float(r[1]); }
DI float x32_sum(float v) { auto r = __builtin_amdgcn_permlane32_swap(__float_as_uint(v), __float_as_uint(v), false, false); return __uint_as_float(r[0]) + __uint_as_float(r[1]); }
DI float x16_max(float v) { auto r = __builtin_amdgcn_permlane16_swap(__float_as_uint(v), __float_as_uint(v), false, false); return fmx(__uint_as_float(r[0]), __uint_as_float(r[1])); }
DI float x32_max(float v) { auto r = __builtin_amdgcn_permlane32_swap(__float_as_uint(v), __float_as_uint(v), false, false); return fmx(__uint_as_float(r[0]), __uint_as_float(r[1])); }
DI float wave_sum(float v) {
    v += dpp<0xB1>(v); v += dpp<0x4E>(v); v += dpp<0x141>(v); v += dpp<0x140>(v);
    return x32_sum(x16_sum(v));
}
DI void red2_rows(float& a, float& b) {
    auto r = __builtin_amdgcn_permlane32_swap(__float_as_uint(a), __float_as_uint(b), false, false);
    float s = __uint_as_float(r[0]) + __uint_as_float(r[1]);
    s = x16_sum(s);
    auto q = __builtin_amdgcn_permlane32_swap(__float_as_uint(s), __float_as_uint(s), false, false);
    a = __uint_as_float(q[0]); b = __uint_as_float(q[1]);
}
DI float sum4(f32x4 v) { return (v[0] + v[1]) + (v[2] + v[3]); }
DI float max4(f32x4 v) { return fmx(fmx3(v[0], v[1], v[2]), v[3]); }
DI unsigned umax4(v4u v) { return max(max(v[0], v[1]), max(v[2], v[3])); }
DI f32x4 asf(v4u v) { return __builtin_bit_cast(f32x4, v); }
#define BAR() do { asm volatile("s_waitcnt lgkmcnt(0)" ::: "memory"); __builtin_amdgcn_s_barrier(); asm volatile("" ::: "memory"); } while (0)
#define FENCE() asm volatile("" ::: "memory")
#define SCHED() __builtin_amdgcn_sched_barrier(0)
#define PIN(v) asm volatile("" : "+v"(v))
typedef __amdgpu_buffer_rsrc_t rsrc_t;
DI rsrc_t mkr(const void* p, int bytes) { return __builtin_amdgcn_make_buffer_rsrc((void*)p, 0, bytes, 0x00020000); }
DI v4u ld128(rsrc_t r, int off, int so = 0) { return __builtin_amdgcn_raw_buffer_load_b128(r, off, so, 16); }
DI v2u ld64(rsrc_t r, int off, int so = 0) { return __builtin_amdgcn_raw_buffer_load_b64(r, off, so, 16); }
DI unsigned ld32(rsrc_t r, int off, int so = 0) { return __builtin_amdgcn_raw_buffer_load_b32(r, off, so, 16); }
DI void st128(rsrc_t r, int off, int so, v4u v) {
    __builtin_amdgcn_raw_buffer_store_b128(v, r, off, so, 16);
    asm volatile("s_nop 1" :: "v"(v) : "memory");
}
DI void st64(rsrc_t r, int off, int so, v2u v) { __builtin_amdgcn_raw_buffer_store_b64(v, r, off, so, 16); }
DI void st32(rsrc_t r, int off, int so, unsigned v) { __builtin_amdgcn_raw_buffer_store_b32(v, r, off, so, 16); }
DI v4u in128(rsrc_t r, int off, int so) { return __builtin_amdgcn_raw_buffer_load_b128(r, off, so, 0); }
DI v2u in64(rsrc_t r, int off, int so) { return __builtin_amdgcn_raw_buffer_load_b64(r, off, so, 0); }
DI unsigned in32(rsrc_t r, int off, int so) { return __builtin_amdgcn_raw_buffer_load_b32(r, off, so, 0); }
DI unsigned perm_lo(unsigned a, unsigned b) { return __builtin_amdgcn_perm(b, a, 0x05040100u); }
DI unsigned perm_hi(unsigned a, unsigned b) { return __builtin_amdgcn_perm(b, a, 0x07060302u); }
DI u64 mktag(v2u ep) {
    const u64 e = ((u64)(unsigned)__builtin_amdgcn_readfirstlane(ep[1]) << 32) | (unsigned)__builtin_amdgcn_readfirstlane(ep[0]);
    return e + 0x9E3779B97F4A7C15ull;
}
DI unsigned now32() { return (unsigned)__builtin_amdgcn_s_memrealtime(); }
DI void wait_until(unsigned t0, unsigned ticks) { while (now32() - t0 < ticks) __builtin_amdgcn_s_sleep(1); }
DI v4u l128(unsigned a) { return *(const LAS v4u*)a; }
DI v4u l128v(unsigned a) { return *(const volatile LAS v4u*)a; }
DI void s128(unsigned a, v4u v) { *(LAS v4u*)a = v; }
DI void s64(unsigned a, v2u v) { *(volatile LAS v2u*)a = v; }
DI void s32(unsigned a, unsigned v) { *(volatile LAS unsigned*)a = v; }
DI void s32f(unsigned a, float v) { *(volatile LAS float*)a = v; }
DI v2u ltr(unsigned a) { return __builtin_bit_cast(v2u, __builtin_amdgcn_ds_read_tr16_b64_v4i16((LAS s16x4*)a)); }
typedef unsigned short u16x2 __attribute__((ext_vector_type(2)));
DI unsigned pksubsat(unsigned a, unsigned short d) { return __builtin_bit_cast(unsigned, __builtin_elementwise_sub_sat(__builtin_bit_cast(u16x2, a), (u16x2){d, d})); }
template <int N> struct IC { static constexpr int value = N; };
#define UNLIKELY(c) __builtin_expect(!!(c), 0)
#define POLL(LOADS, BAD, SLEEP, SH) do { LOADS; if (UNLIKELY(BAD)) { unsigned spins = 0; _Pragma("nounroll") do { __builtin_amdgcn_s_sleep(SLEEP); FENCE(); LOADS; ++spins; } while ((BAD) && spins < SPIN_CAP); COUNT(SH); } } while (0)
constexpr int OOB = 0x7ffffff0;
DI v4u sel4(unsigned d, v4u m) { return (v4u){d & m[0], d & m[1], d & m[2], d & m[3]}; }

#define STAMP(i)
#define STAMP_OUT()
#define COUNT(sh)

__global__ void __launch_bounds__(NT, 3) fused_k(Params p) {
    extern __shared__ __attribute__((aligned(16))) unsigned char lds_[];
    LAS unsigned char* L = (LAS unsigned char*)lds_;
    const unsigned LB = (unsigned)(uintptr_t)L;
    u64 pc0;
    asm volatile("s_getpc_b64 %0" : "=s"(pc0));
    const int b = blockIdx.x, tid = threadIdx.x, l = tid & 63, w = __builtin_amdgcn_readfirstlane(tid >> 6);
    const int g = l >> 4, i16 = l & 15;
    const int x = b & 7, i = b >> 3, isW = i & 1, pi = i >> 1, kc = x * 16 + pi, hh = isW;
    const rsrc_t rs = mkr(p.ws, W_END);
    const unsigned t0 = now32();
    const v4u ones = {SENT, SENT, SENT, SENT};
    STAMP(0);
    if (w >= 8 && !isW) {
        const unsigned ln = (pi * 4 + w - 8) * 320;
        if (ln < CODE_KB * 1024) {
            const u64 ca = (pc0 & ~63ull) + ln;
            unsigned c0, c1, c2, c3, c4;
            asm volatile("s_load_dword %0, %5, 0x0\n\ts_load_dword %1, %5, 0x40\n\ts_load_dword %2, %5, 0x80\n\ts_load_dword %3, %5, 0xc0\n\ts_load_dword %4, %5, 0x100\n\ts_waitcnt lgkmcnt(0)"
                         : "=&s"(c0), "=&s"(c1), "=&s"(c2), "=&s"(c3), "=&s"(c4) : "s"(ca) : "memory");
        }
    }

    if (w >= 8) {
    const int cw = w - 8;
    __builtin_amdgcn_s_setprio(3);
    const v2u ep = ld64(rs, W_EP);
#define CLEAR() { _Pragma("unroll") for (int n = 0; n < NCLR; ++n) st128(rs, l * 16, W_OB + kc * 32768 + cw * 8192 + n * 1024, ones); \
        st128(rs, l < 32 ? l * 16 : OOB, W_LS + kc * 2048 + cw * 512, ones); \
        asm volatile("s_waitcnt vmcnt(0)" ::: "memory"); \
        st64(rs, l == 0 ? 0 : OOB, W_FL + kc * 128 + cw * 8, tag); }
    if (cw == 2) {
        s128(LB + L_MX + l * 16, ones);
        if (l < 36) s128(LB + L_MX + 1024 + l * 16, ones);
    }
    const u64 tag64 = mktag(ep);
    const v2u tag = {(unsigned)tag64, (unsigned)(tag64 >> 32)};
    if (!isW) { wait_until(t0, T_CLR); CLEAR() }
    STAMP(1);
    if (isW) { BAR(); BAR(); }
    {
        const int qb = W_QA + (16 * hh + 4 * cw) * 2048;
        int qv = l * 16;
        unsigned qw = LB + L_QA + 4 * cw * RS + l * 8;
        PIN(qv); PIN(qw);
        wait_until(t0, T_QA);
        STAMP(3);
        v4u q[8];
        unsigned bad;
#define QALOAD _Pragma("unroll") for (int n = 0; n < 8; ++n) q[n] = ld128(rs, qv, qb + n * 1024); \
        bad = 0; _Pragma("unroll") for (int n = 0; n < 8; ++n) { bad |= (q[n][1] ^ tag[1]) | (q[n][3] ^ tag[1]); s64(qw + (n >> 1) * RS + (n & 1) * 512, (v2u){q[n][0], q[n][2]}); }
        POLL(QALOAD, !__all(bad == 0), 4, 28);
    }
    STAMP(4);
    BAR();
    const int vh = 4 * x + (i >> 3), vs = i & 7;
    v4u uvr[4];
    {
        v4u f = ld128(rs, (l >> 1) * 128 + (l & 1) * 16, W_FL + 32 * cw * 128);
        const rsrc_t ru = mkr(p.wuv, 4 << 20);
#pragma unroll
        for (int n = 0; n < 4; ++n) uvr[n] = in128(ru, (l >> 2) * 8192 + (l & 3) * 16, (64 * vs + 16 * n) * 8192 + vh * 256 + cw * 64);
        SCHED();
#define FLBAD !__all((f[0] == tag[0]) & (f[1] == tag[1]) & (f[2] == tag[0]) & (f[3] == tag[1]))
        if (UNLIKELY(FLBAD)) {
            unsigned spins = 0;
            _Pragma("nounroll") do { __builtin_amdgcn_s_sleep(4); FENCE(); f = ld128(rs, (l >> 1) * 128 + (l & 1) * 16, W_FL + 32 * cw * 128); } while ((FLBAD) && ++spins < SPIN_CAP);
        }
        s32(LB + L_OK + cw * 4, 0u);
    }
    STAMP(2);
    __builtin_amdgcn_s_setprio(0);
    auto tail = [&](auto role_) __attribute__((always_inline)) {
    constexpr int ROLE = decltype(role_)::value;
    constexpr bool isV = ROLE == 1, isF = ROLE == 2;
    const int fc = 7 * x + pi - 8;
    v4u wf[16];
    unsigned xb = 0, b1v = 0; v2u b2v = {0, 0};
#define TRANSPOSE8(dst, src) _Pragma("unroll") for (int t = 0; t < 4; ++t) _Pragma("unroll") for (int c = 0; c < 4; ++c) { \
        (dst)[2 * c][t] = perm_lo((src)[2 * t][c], (src)[2 * t + 1][c]); (dst)[2 * c + 1][t] = perm_hi((src)[2 * t][c], (src)[2 * t + 1][c]); }
    if (isV) {
        const rsrc_t ro = mkr(p.wo, 4 << 20);
#pragma unroll
        for (int n = 0; n < 16; ++n) wf[n] = in128(ro, (i16 >> 3) * 32768 + g * 4096 + (i16 & 7) * 16 + ((n & 7) >> 1) * 1024, ((4 * x + cw) * 128 + 64 * (n >> 3) + 16 * (n & 1)) * 1024 + pi * 128);
    } else if (isF) {
        const rsrc_t r1 = mkr(p.w1, 512 * 1792 * 2), r2 = mkr(p.w2, 512 * 1792 * 2);
#pragma unroll
        for (int n = 0; n < 8; ++n) wf[n] = in128(r1, (l >> 2) * 3584 + (l & 3) * 16, (128 * cw + 16 * n) * 3584 + fc * 64);
#pragma unroll
        for (int j = 0; j < 8; ++j) wf[8 + j] = in128(r2, g * 8192 + i16 * 16 + (j & 3) * 1024, (32 * fc + (j & 4)) * 1024 + cw * 256);
        xb = *(const GAS unsigned*)(p.x + 128 * cw + 2 * l);
        b1v = p.b1[32 * fc + 8 * g + (i16 & 7)];
        b2v = *(const GAS v2u*)(p.b2 + 32 * (2 * x + (pi & 1)) + 8 * (l & 3) + 4 * ((l >> 2) & 1));
    }
    SCHED();
    {
        v4u selm;
#pragma unroll
        for (int t = 0; t < 4; ++t) selm[t] = (((l & 7) >> 1) == t) ? ((l & 1) ? 0xffff0000u : 0x0000ffffu) : 0u;
        int lsv = (4 * (l >> 3) + (l & 3)) * 128, obv = (l >> 3) * 512 + (l & 7) * 16;
        const int lss = W_LS + ((vh >> 4) * 128 + 32 * cw) * 128 + (vh & 15) * 8, obs = W_OB + (vh * 8 + vs) * 16384 + cw * 4096;
        unsigned wp = LB + L_SC + ((g >> 1) == (i16 >> 3) ? 2 * cw + (g >> 1) : 10 + cw) * 144 + (8 * (i16 & 7) + 4 * (g & 1)) * 2;
        unsigned rp = LB + L_SC + (i16 & 7) * 144 + 16 * g;
        unsigned sp = LB + L_SC + 1280 + cw * 4, sq = LB + L_SC + 1280;
        int cxv = l < 16 ? l * 4 : OOB;
        PIN(selm); PIN(lsv); PIN(obv); PIN(wp); PIN(rp); PIN(sp); PIN(sq); PIN(cxv);
        SCHED();
        STAMP(5);
        BAR();
        const unsigned tB = now32();
        v4u uvf[2][2];
        {
            const unsigned ub = LB + L_CT + cw * 8192;
#pragma unroll
            for (int n = 0; n < 4; ++n) s128(ub + (16 * n + (l >> 2)) * 80 + (l & 3) * 16, uvr[n]);
            const unsigned ur = ub + (16 * (i16 >> 2) + g) * 80 + (i16 & 3) * 8;
#pragma unroll
            for (int tl = 0; tl < 2; ++tl)
#pragma unroll
                for (int ks = 0; ks < 2; ++ks) {
                    const v2u a_ = ltr(ur + (4 * ks) * 80 + tl * 32), c_ = ltr(ur + (4 * ks + 8) * 80 + tl * 32);
                    uvf[tl][ks] = (v4u){a_[0], a_[1], c_[0], c_[1]};
                }
            PIN(uvf[0][0]); PIN(uvf[0][1]); PIN(uvf[1][0]); PIN(uvf[1][1]);
        }
        if (isV) {
#pragma unroll
            for (int hb = 0; hb < 2; ++hb) {
                v4u r[8];
#pragma unroll
                for (int n = 0; n < 8; ++n) r[n] = wf[8 * hb + n];
                TRANSPOSE8(wf + 8 * hb, r)
#pragma unroll
                for (int n = 0; n < 8; ++n) PIN(wf[8 * hb + n]);
                SCHED();
            }
        }
        SCHED();
        wait_until(tB, DLY_B);
        STAMP(10);
        f32x4 acc;
        unsigned mx;
#define V1TRY { const v2u ml = ld64(rs, lsv, lss); v4u od[4]; \
            _Pragma("unroll") for (int n = 0; n < 4; ++n) od[n] = ld128(rs, obv + n * 128, obs); \
            SCHED(); \
            const float m = __uint_as_float(ml[0]); \
            float M = fmx3(m, dpp<0xB1>(m), dpp<0x4E>(m)); \
            M = fmx(M, dpp<0xB1>(M)); \
            M = x32_max(x16_max(fmx(M, dpp<0x128>(M)))); \
            const float e = __builtin_amdgcn_exp2f(m - M); \
            const unsigned dup = pk(e, e); \
            const v4u a0 = sel4(dppu<0x00>(dup), selm), a1 = sel4(dppu<0x55>(dup), selm), a2 = sel4(dppu<0xAA>(dup), selm), a3 = sel4(dppu<0xFF>(dup), selm); \
            float Lw = bflo(dup) * __uint_as_float(ml[1]); \
            Lw += dpp<0xB1>(Lw); Lw += dpp<0x4E>(Lw); Lw += dpp<0x128>(Lw); \
            Lw = x32_sum(x16_sum(Lw)); \
            s32f(sp, M); s32f(sp + 16, Lw); \
            mx = max(ml[0], ml[1]); \
            SCHED(); \
            acc = mfma(a0, od[0], (f32x4){0.f, 0.f, 0.f, 0.f}); acc = mfma(a1, od[1], acc); acc = mfma(a2, od[2], acc); acc = mfma(a3, od[3], acc); \
            _Pragma("unroll") for (int n = 0; n < 4; ++n) mx = max(mx, umax4(od[n])); }
        POLL(V1TRY, !__all(mx != SENT), 1, 0);
        STAMP(6);
        s64(wp, (v2u){pk(acc[0], acc[1]), pk(acc[2], acc[3])});
        BAR();
        const v4u a0 = l128(rp), a1 = l128(rp + 64);
        const f32x4 Ms = asf(l128(sq)), Ls = asf(l128(sq + 16));
        f32x4 d0 = {0.f, 0.f, 0.f, 0.f}, d1 = {0.f, 0.f, 0.f, 0.f};
        d0 = mfma(a0, uvf[0][0], d0); d1 = mfma(a0, uvf[1][0], d1);
        d0 = mfma(a1, uvf[0][1], d0); d1 = mfma(a1, uvf[1][1], d1);
        const float Mg = max4(Ms);
        const f32x4 fw = {__builtin_amdgcn_exp2f(Ms[0] - Mg), __builtin_amdgcn_exp2f(Ms[1] - Mg), __builtin_amdgcn_exp2f(Ms[2] - Mg), __builtin_amdgcn_exp2f(Ms[3] - Mg)};
        const float Lr = __builtin_amdgcn_rcpf(sum4(fw * Ls));
        const float fa = ((g & 1) ? fw[2] : fw[0]) * Lr, fb = ((g & 1) ? fw[3] : fw[1]) * Lr;
        const float r0 = x16_sum(fa * (d0[0] + d0[1]) + fb * (d0[2] + d0[3])), r1 = x16_sum(fa * (d1[0] + d1[1]) + fb * (d1[2] + d1[3]));
        st32(rs, cxv, W_CX + (vh * 8 + vs) * 256 + cw * 64, san(pk(r0, r1)));
    }
    const unsigned tV = now32();
    STAMP(7);
    if (isV) {
        const int cbs = W_CX + (4 * x + cw) * 2048;
        int cbv = l * 16, opv = l < 16 ? l * 16 : OOB;
        unsigned wp = LB + L_CT + cw * 4096 + (l >> 4) * 272 + i16 * 16;
        unsigned rp = LB + L_CT + cw * 4096 + (i16 & 7) * 272 + (i16 >> 3) * 64 + g * 16;
        unsigned ow = LB + L_QA + ((g >> 1) == (i16 >> 3) ? 2 * cw + (g >> 1) : 8 + cw) * 272 + (i16 & 7) * 32 + (g & 1) * 16, orr = LB + L_QA + g * 544 + i16 * 16;
        PIN(cbv); PIN(opv); PIN(wp); PIN(rp); PIN(ow); PIN(orr);
#pragma unroll
        for (int t = 0; t < 16; ++t) PIN(wf[t]);
        SCHED();
        wait_until(tV, DLY_V);
        v4u c0, c1;
        POLL(c0 = ld128(rs, cbv, cbs); c1 = ld128(rs, cbv + 1024, cbs), !__all(max(umax4(c0), umax4(c1)) != SENT), 1, 8);
        STAMP(8);
        s128(wp, c0); s128(wp + 4 * 272, c1);
        const v4u cx0 = l128(rp), cx1 = l128(rp + 128);
        f32x4 d[8];
#pragma unroll
        for (int t = 0; t < 8; ++t) d[t] = mfma(cx0, wf[t], (f32x4){0.f, 0.f, 0.f, 0.f});
#pragma unroll
        for (int t = 0; t < 8; ++t) d[t] = mfma(cx1, wf[8 + t], d[t]);
        v4u e;
#pragma unroll
        for (int t = 0; t < 4; ++t) {
            auto s = __builtin_amdgcn_permlane16_swap(__float_as_uint(sum4(d[t])), __float_as_uint(sum4(d[t + 4])), false, false);
            e[t] = __float_as_uint(__uint_as_float(s[0]) + __uint_as_float(s[1]));
        }
        s128(ow, e);
        BAR();
        if (cw == 0) {
            const f32x4 s = asf(l128(orr)) + asf(l128(orr + 272));
            float s0 = s[0], s1 = s[1], s2 = s[2], s3 = s[3];
            red2_rows(s0, s1); red2_rows(s2, s3);
            st128(rs, opv, W_OP + x * 2048 + pi * 256, (v4u){san(__float_as_uint(s0)), san(__float_as_uint(s1)), san(__float_as_uint(s2)), san(__float_as_uint(s3))});
        }
        STAMP(13);
        STAMP_OUT();
        return;
    }
    if (!isF) { STAMP_OUT(); return; }
    {
        const unsigned ub = LB + L_CT + 32768 + cw * 10240;
#pragma unroll
        for (int n = 0; n < 8; ++n) s128(ub + (16 * n + (l >> 2)) * 80 + (l & 3) * 16, wf[n]);
        const unsigned ur = ub + (8 * g + (i16 >> 2)) * 80 + (i16 & 3) * 8;
#pragma unroll
        for (int tl = 0; tl < 2; ++tl)
#pragma unroll
            for (int ks = 0; ks < 4; ++ks) {
                const v2u a_ = ltr(ur + (32 * ks) * 80 + tl * 32), c_ = ltr(ur + (32 * ks + 4) * 80 + tl * 32);
                wf[4 * tl + ks] = (v4u){a_[0], a_[1], c_[0], c_[1]};
            }
        v4u q[8];
#pragma unroll
        for (int n = 0; n < 8; ++n) q[n] = wf[8 + n];
        TRANSPOSE8(wf + 8, q)
    }
    {
        const v4u one = {0x3f803f80u, 0x3f803f80u, 0x3f803f80u, 0x3f803f80u};
        f32x4 d0 = {0.f, 0.f, 0.f, 0.f}, d1 = {0.f, 0.f, 0.f, 0.f};
#pragma unroll
        for (int ks = 0; ks < 4; ++ks) { d0 = mfma(one, wf[ks], d0); d1 = mfma(one, wf[4 + ks], d1); }
        s32f(LB + L_SC + 2560 + i16 * 16 + cw * 4, d0[0]); s32f(LB + L_SC + 2560 + (16 + i16) * 16 + cw * 4, d1[0]);
    }
    {
        int opv = l * 8;
        unsigned hw = LB + L_P + cw * 544 + l * 4, hr = LB + L_P + cw * 544 + (i16 & 1) * 272 + g * 16;
        unsigned hf = LB + L_QA + (128 * cw + 2 * l) * 4;
        unsigned uw = LB + L_SC + 3072 + i16 * 16 + cw * 4, ur = LB + L_SC + 2560 + (8 * g + (i16 & 7)) * 16;
        unsigned sw = LB + L_SC + 3584 + cw * 4, sr = LB + L_SC + 3584;
        int ypv = (g & 1) ? OOB : (i16 >> 2) * (56 * 128) + (i16 & 3) * 16 + (g >> 1) * 64;
        float x0 = bflo(xb), x1 = bfhi(xb), b1f = bflo(b1v);
        PIN(opv); PIN(hw); PIN(hr); PIN(hf); PIN(uw); PIN(ur); PIN(sw); PIN(sr); PIN(ypv); PIN(x0); PIN(x1); PIN(b1f);
#pragma unroll
        for (int t = 0; t < 16; ++t) PIN(wf[t]);
        SCHED();
        wait_until(tV, DLY_C);
        v2u op[8];
        unsigned mx;
#define OPLOAD _Pragma("unroll") for (int n = 0; n < 8; ++n) op[n] = ld64(rs, opv, W_OP + n * 2048 + cw * 512); \
        mx = 0; _Pragma("unroll") for (int n = 0; n < 8; ++n) mx = max(mx, max(op[n][0], op[n][1]));
        POLL(OPLOAD, !__all(mx != SENT), 1, 8);
        STAMP(8);
        f32x2 h = {x0, x1};
#pragma unroll
        for (int n = 0; n < 8; ++n) h += __builtin_bit_cast(f32x2, op[n]);
        const float mw = wave_sum(h[0] + h[1]) * (1.f / 128.f);
        const float c0 = h[0] - mw, c1 = h[1] - mw;
        const unsigned hi = pk(c0, c1), lo = pk(c0 - bflo(hi), c1 - bfhi(hi));
        s32(hw, hi); s32(hw + 272, lo);
        v4u a[4];
#pragma unroll
        for (int ks = 0; ks < 4; ++ks) a[ks] = l128v(hr + ks * 64);
        s64(hf, __builtin_bit_cast(v2u, h));
        SCHED();
        const float m2 = wave_sum(c0 * c0 + c1 * c1);
        f32x4 d0 = {0.f, 0.f, 0.f, 0.f}, d1 = {0.f, 0.f, 0.f, 0.f};
#pragma unroll
        for (int ks = 0; ks < 4; ++ks) { d0 = mfma(a[ks], wf[ks], d0); d1 = mfma(a[ks], wf[4 + ks], d1); }
        s32f(sw, mw); s32f(sw + 16, m2);
        s32f(uw, d0[0] + d0[1]); s32f(uw + 256, d1[0] + d1[1]);
        BAR();
        const f32x4 mm = asf(l128(sr)), qq = asf(l128(sr + 16));
        const f32x4 cc = asf(l128(ur)), uu = asf(l128(ur + 512));
        const float mean = sum4(mm) * 0.25f;
        const f32x4 dm = mm - mean;
        const float var = (sum4(qq) + 128.f * sum4(dm * dm)) * (1.f / 512.f);
        const float rstd = __builtin_amdgcn_rsqf(var + 1e-5f);
        const float u = (sum4(uu) + sum4(dm * cc)) * rstd + b1f;
        const float z = u * (1.f + 0.044715f * u * u) * (-2.f * 0.7978845608028654f * 1.4426950408889634f);
        const float ge = u * __builtin_amdgcn_rcpf(1.f + __builtin_amdgcn_exp2f(z));
        const unsigned pr = pk(ge, dpp<0x101>(ge));
        const v4u fa = {pr, dppu<0x102>(pr), dppu<0x104>(pr), dppu<0x106>(pr)};
        float y[8];
#pragma unroll
        for (int t = 0; t < 8; ++t) { f32x4 d = {0.f, 0.f, 0.f, 0.f}; d = mfma(fa, wf[8 + t], d); y[t] = d[0]; }
        v4u v;
#pragma unroll
        for (int t = 0; t < 4; ++t) v[t] = san(__float_as_uint(g ? y[4 + t] : y[t]));
        st128(rs, ypv, W_YP + (4 * cw * 56 + fc) * 128, v);
    }
    const unsigned tF = now32();
    STAMP(13);
    if (pi < 10 && cw == 0) {
        const int fin = 2 * x + (pi & 1);
        const int col = 32 * fin + 8 * (l & 3) + 4 * ((l >> 2) & 1);
        f32x4 hv = asf(l128(LB + L_QA + col * 4));
        hv += (f32x4){bflo(b2v[0]), bfhi(b2v[0]), bflo(b2v[1]), bfhi(b2v[1])};
        const rsrc_t rout = mkr(p.out, 1024);
        int yv_ = l * 16, ov = l < 8 ? col * 2 : OOB;
        PIN(hv); PIN(yv_); PIN(ov);
        SCHED();
        wait_until(tF, DLY_D);
        v4u yv[7];
        unsigned mx;
#define YLOAD _Pragma("unroll") for (int n = 0; n < 7; ++n) yv[n] = ld128(rs, yv_, W_YP + fin * 56 * 128 + n * 1024); \
        mx = 0; _Pragma("unroll") for (int n = 0; n < 7; ++n) mx = max(mx, umax4(yv[n]));
        POLL(YLOAD, !__all(mx != SENT), 1, 16);
        STAMP(12);
        f32x4 s = asf(yv[0]);
#pragma unroll
        for (int n = 1; n < 7; ++n) s += asf(yv[n]);
        float s0 = s[0], s1 = s[1], s2 = s[2], s3 = s[3];
        red2_rows(s0, s1); red2_rows(s2, s3);
        s0 += dpp<0x128>(s0); s1 += dpp<0x128>(s1); s2 += dpp<0x128>(s2); s3 += dpp<0x128>(s3);
        __builtin_amdgcn_raw_buffer_store_b64((v2u){pk(hv[0] + s0, hv[1] + s1), pk(hv[2] + s2, hv[3] + s3)}, rout, ov, 0, 0);
        if (fin == 0) st64(rs, l == 0 ? 0 : OOB, W_EP, tag);
    }
    STAMP_OUT();
    };
    if (!isW && pi < 8) { tail(IC<1>{}); __builtin_amdgcn_endpgm(); }
    if (!isW && pi < 15) { tail(IC<2>{}); __builtin_amdgcn_endpgm(); }
    tail(IC<0>{});
    __builtin_amdgcn_endpgm();
    }
    {
        const int hq = x * 4 + (i >> 3), cq = (i & 7) >> 1;
        unsigned tag32 = 0;
#define UKDMA() { const rsrc_t rk = mkr(p.wuk, 4 << 20); _Pragma("unroll") for (int n = 0; n < 4; ++n) \
            __builtin_amdgcn_raw_ptr_buffer_load_lds(rk, (LAS void*)(L + L_CT + 16 * w * RS + n * 1024), 16, g * 8192 + ((i16 ^ (4 * n + g)) * 16), \
                                                     hq * 256 + (128 * cq + 16 * w + 4 * n) * 8192, 0, 0); }
        if (__builtin_expect(w < 2, 1)) {
            const rsrc_t rq = mkr(p.wq, 4 << 20), rx = mkr(p.x, 1024);
            const v4u xs = in128(rx, l * 16, 0);
            const v4u xr = in128(rx, g * 16, (i & 7) * 128 + w * 64);
            const v2u ep = ld64(rs, W_EP);
            SCHED();
            v4u wq[8];
#pragma unroll
            for (int n = 0; n < 8; ++n) wq[n] = in128(rq, g * 65536 + i16 * 16, hq * 256 + (64 * (i & 7) + 32 * w + n) * 8192);
            SCHED();
            v4u selm;
#pragma unroll
            for (int t = 0; t < 4; ++t) selm[t] = (((l & 7) >> 1) == t) ? ((l & 1) ? 0xffff0000u : 0x0000ffffu) : 0u;
            int qpv = l < 32 ? l * 16 : OOB;
            float xv[8];
#pragma unroll
            for (int t = 0; t < 4; ++t) { xv[2 * t] = bflo(xs[t]); xv[2 * t + 1] = bfhi(xs[t]); }
            float sm = 0.f;
#pragma unroll
            for (int t = 0; t < 8; ++t) sm += xv[t];
            const float mean = wave_sum(sm) * (1.f / 512.f);
            float sq = 0.f;
#pragma unroll
            for (int t = 0; t < 8; ++t) { const float d = xv[t] - mean; sq += d * d; }
            const float rstd = __builtin_amdgcn_rsqf(wave_sum(sq) * (1.f / 512.f) + 1e-5f);
            v4u xa[8];
#pragma unroll
            for (int n = 0; n < 8; ++n) {
                const float xn = (((n & 1) ? bfhi(xr[n >> 1]) : bflo(xr[n >> 1])) - mean) * rstd;
                xa[n] = sel4(pk(xn, xn), selm);
            }
            tag32 = (unsigned)(mktag(ep) >> 32);
            PIN(qpv);
            SCHED();
            f32x4 acc = {0.f, 0.f, 0.f, 0.f};
#pragma unroll
            for (int n = 0; n < 8; ++n) acc = mfma(xa[n], wq[n], acc);
            st128(rs, qpv, W_QP + (hq * 16 + (i & 7) * 2 + w) * 512, (v4u){pk(acc[0], acc[1]), tag32, pk(acc[2], acc[3]), tag32});
            STAMP(1);
        }
        if (isW) {
            UKDMA()
            if (w >= 2) { const v2u ep = ld64(rs, W_EP); tag32 = (unsigned)(mktag(ep) >> 32); }
            const int kr = 4 * (i16 & 3) + (i16 >> 2);
            unsigned wp = LB + L_SC + (2 * w + (l >> 5)) * 272 + i16 * 16 + (g & 1) * 8;
            unsigned rp = LB + L_SC + i16 * 272 + 16 * g;
            unsigned kp = LB + L_CT + 16 * w * RS + kr * 256;
            int qv = l * 16, qav = l < 4 ? l * 16 : OOB;
            PIN(wp); PIN(rp); PIN(qv); PIN(qav); PIN(kp);
            SCHED();
            wait_until(t0, T_QP);
            v4u qg;
            POLL(qg = ld128(rs, qv, W_QP + (hq * 16 + 2 * w) * 512), !__all((qg[1] == tag32) & (qg[3] == tag32)), 1, 24);
            s64(wp, (v2u){qg[0], qg[2]});
            v4u uk[4];
#pragma unroll
            for (int s = 0; s < 4; ++s) uk[s] = l128v(kp + (((4 * s + g) ^ kr) * 16));
            STAMP(9);
            BAR();
            v4u a[4];
#pragma unroll
            for (int s = 0; s < 4; ++s) a[s] = l128(rp + 64 * s);
            f32x4 d0 = {0.f, 0.f, 0.f, 0.f};
#pragma unroll
            for (int s = 0; s < 4; ++s) d0 = mfma(a[s], uk[s], d0);
            constexpr float QS = 0.08838834764831845f * 1.4426950408889634f;
            const float r0 = x32_sum(x16_sum(sum4(d0))) * QS;
            const unsigned pr = pk(r0, dpp<0x104>(r0)), pr2 = dppu<0x108>(pr);
            st128(rs, qav, W_QA + hq * 2048 + cq * 512 + w * 64, (v4u){pr, tag32, pr2, tag32});
            STAMP(2);
            BAR();
        } else {
            wait_until(t0, T_C);
        }
        const int wk = w;
        {
            const rsrc_t rc = mkr(p.ckv, 16 << 20);
#pragma unroll
            for (int r = 0; r < 16; ++r)
                __builtin_amdgcn_raw_ptr_buffer_load_lds(rc, (LAS void*)(L + L_CT + (16 * w + r) * RS), 16, l * 16, (kc * 128 + 16 * w + r) * 1024, 0, 0);
        }
        const int rho = 8 * (i16 >> 3) + 2 * (i16 & 3) + ((i16 >> 2) & 1);
        unsigned qp = LB + L_QA + i16 * RS + g * 256;
        unsigned cp = LB + L_CT + (16 * wk + rho) * RS + g * 256;
        unsigned tp0 = LB + L_CT + (8 * g + 2 * (i16 >> 2)) * RS + 128 * w + 8 * (i16 & 3);
        unsigned tp1 = tp0 + 64 * RS;
        unsigned mo = LB + L_MX + i16 * 48 + (g >> 1) * 16, mx_ = LB + L_MX + i16 * 48 + (1 - (g >> 1)) * 16;
        unsigned mq = LB + L_MX + i16 * 48 + (wk & 1) * 16 + (wk >> 1) * 4;
        unsigned pw = LB + L_P + i16 * PS + (16 * wk + 4 * g) * 2;
        unsigned pr = LB + L_P + i16 * PS + g * 16;
        int obv = (4 * g + 2 * (i16 >> 3)) * (8 * 16384) + (i16 & 7) * 16;
        unsigned okp = LB + L_OK;
        PIN(qp); PIN(cp); PIN(tp0); PIN(tp1); PIN(mo); PIN(mx_); PIN(mq); PIN(pw); PIN(pr); PIN(obv); PIN(okp);
        STAMP(3);
        BAR();
        STAMP(4);
        v4u qf[16], cr[16];
#pragma unroll
        for (int s = 0; s < 16; ++s) qf[s] = l128(qp + s * 16);
        SCHED();
        __builtin_amdgcn_s_waitcnt(0x0F70);
        FENCE();
        STAMP(5);
#pragma unroll
        for (int s = 0; s < 16; ++s) cr[s] = l128(cp + s * 16);
        SCHED();
        f32x4 sc = {0.f, 0.f, 0.f, 0.f};
#pragma unroll
        for (int s = 0; s < 16; ++s) sc = mfma(cr[s], qf[s], sc);
        const float mw = __builtin_ceilf(x32_max(x16_max(max4(sc))));
        {
            const unsigned p0 = pk(__builtin_amdgcn_exp2f(sc[0] - mw), __builtin_amdgcn_exp2f(sc[1] - mw)), p1 = pk(__builtin_amdgcn_exp2f(sc[2] - mw), __builtin_amdgcn_exp2f(sc[3] - mw));
            s64(pw, (v2u){p0, p1});
            const bf16x2 one2 = __builtin_bit_cast(bf16x2, 0x3f803f80u);
            float ls = __builtin_amdgcn_fdot2_f32_bf16(__builtin_bit_cast(bf16x2, p0), one2, 0.f, false);
            ls = __builtin_amdgcn_fdot2_f32_bf16(__builtin_bit_cast(bf16x2, p1), one2, ls, false);
            s32(mq, san(__float_as_uint(mw)));
            ls = x32_sum(x16_sum(ls));
            s32(mq + 768, san(__float_as_uint(ls)));
        }
        STAMP(6);
        v4u m0, m1, pf[4];
#define MXLOAD m0 = l128v(mo); m1 = l128v(mx_); _Pragma("unroll") for (int ks = 0; ks < 4; ++ks) pf[ks] = l128v(pr + ks * 64)
        POLL(MXLOAD, !__all(max(umax4(m0), umax4(m1)) != SENT), 1, 0);
        FENCE();
        STAMP(7);
        v4u cf[4][4];
#define TRREAD(ks) _Pragma("unroll") for (int ct = 0; ct < 4; ++ct) { \
            const unsigned t_ = ((ks) < 2 ? tp0 : tp1) + (32 * ((ks) & 1)) * RS + 32 * ct; \
            const v2u a_ = ltr(t_), c_ = ltr(t_ + RS); \
            cf[ks][ct] = (v4u){a_[0], a_[1], c_[0], c_[1]}; }
        TRREAD(0) TRREAD(1)
        v4u okw = l128v(okp);
        SCHED();
        const float M = fmx(max4(asf(m0)), max4(asf(m1)));
#pragma unroll
        for (int ks = 0; ks < 4; ++ks) {
            const unsigned short dl = (unsigned short)(unsigned)__builtin_fminf((M - __uint_as_float(m0[ks])) * 128.f, 32640.f);
#pragma unroll
            for (int t = 0; t < 4; ++t) pf[ks][t] = pksubsat(pf[ks][t], dl);
        }
        SCHED();
        TRREAD(2) TRREAD(3)
        SCHED();
        f32x4 o[4];
#pragma unroll
        for (int ct = 0; ct < 4; ++ct) o[ct] = (f32x4){0.f, 0.f, 0.f, 0.f};
#pragma unroll
        for (int ks = 0; ks < 2; ++ks)
#pragma unroll
            for (int ct = 0; ct < 4; ++ct) o[ct] = mfma(pf[ks], cf[ks][ct], o[ct]);
        SCHED();
        if (UNLIKELY(__builtin_amdgcn_readfirstlane(umax4(okw)) == SENT)) {
            unsigned spins = 0;
            _Pragma("nounroll") do { __builtin_amdgcn_s_sleep(1); okw = l128v(okp); } while (__builtin_amdgcn_readfirstlane(umax4(okw)) == SENT && ++spins < SPIN_CAP);
        }
        FENCE();
        SCHED();
#pragma unroll
        for (int ks = 2; ks < 4; ++ks)
#pragma unroll
            for (int ct = 0; ct < 4; ++ct) o[ct] = mfma(pf[ks], cf[ks][ct], o[ct]);
        STAMP(11);
        const int obs = W_OB + (16 * hh * 8 + w) * 16384 + kc * 128;
#pragma unroll
        for (int r = 0; r < 2; ++r) {
            v4u v;
#pragma unroll
            for (int t = 0; t < 2; ++t) {
                const unsigned A = san(pk(o[2 * t][r], o[2 * t + 1][r])), B = san(pk(o[2 * t][r + 2], o[2 * t + 1][r + 2]));
                v[t] = (unsigned)__builtin_amdgcn_update_dpp((int)A, (int)B, 0x128, 0xf, 0xC, false);
                v[2 + t] = (unsigned)__builtin_amdgcn_update_dpp((int)B, (int)A, 0x128, 0xf, 0x3, false);
            }
            st128(rs, obv, obs + r * (8 * 16384), v);
        }
        if (w == 0) {
            v4u l0, l1;
            POLL(l0 = l128v(mo + 768); l1 = l128v(mx_ + 768), !__all(max(umax4(l0), umax4(l1)) != SENT), 1, 0);
            float lt = 0.f;
#pragma unroll
            for (int t = 0; t < 4; ++t) lt += __uint_as_float(l0[t]) * __builtin_amdgcn_exp2f(__uint_as_float(m0[t]) - M) + __uint_as_float(l1[t]) * __builtin_amdgcn_exp2f(__uint_as_float(m1[t]) - M);
            st64(rs, l < 16 ? l * 8 : OOB, W_LS + (hh * 128 + kc) * 128, (v2u){san(__float_as_uint(M)), san(__float_as_uint(lt))});
        }
        STAMP(8);
        STAMP_OUT();
    }
}

extern "C" void kernel_launch(void* const* d_in, const int* in_sizes, int n_in, void* d_out, int out_size, void* d_ws, size_t ws_size,
                              hipStream_t stream) {
    Params p;
    p.x = (const u16*)d_in[0]; p.ckv = (const u16*)d_in[1]; p.wq = (const u16*)d_in[2]; p.wuk = (const u16*)d_in[3];
    p.wuv = (const u16*)d_in[4]; p.wo = (const u16*)d_in[5]; p.w1 = (const u16*)d_in[7]; p.b1 = (const u16*)d_in[8];
    p.w2 = (const u16*)d_in[9]; p.b2 = (const u16*)d_in[10]; p.out = (u16*)d_out; p.ws = (unsigned char*)d_ws + WS_OFF(ws_size);
    (void)hipFuncSetAttribute((const void*)fused_k, hipFuncAttributeMaxDynamicSharedMemorySize, L_END);
    hipLaunchKernelGGL(fused_k, dim3(NWG), dim3(NT), L_END, stream, p);
}
```
